```python
import torch

def _softmax(x, axis=-1):
    return torch.softmax(x, dim=axis)
def _gelu(x):
    return torch.nn.functional.gelu(x, approximate="tanh")
def _silu(x):
    return torch.nn.functional.silu(x)
def _sigmoid(x):
    return torch.sigmoid(x)
def _layernorm(x, axis=-1, eps=1e-5):
    m = x.mean(dim=axis, keepdim=True)
    v = x.var(dim=axis, keepdim=True, unbiased=False)
    return (x - m) / torch.sqrt(v + eps)
def _scatter_add(tgt, idx, src, axis=0):
    return tgt.clone().index_add_(axis, idx, src)
def _segment_sum(src, seg, n):
    out = torch.zeros((n,) + src.shape[1:], dtype=src.dtype)
    return out.index_add_(0, seg, src)
def _pad(x, pw):
    flat = tuple(v for pair in reversed(pw) for v in pair)
    return torch.nn.functional.pad(x, flat)

kernel_name = 'concat_heads__mha_block__mha_block__prompt'

def setup_inputs():

    _w = (lambda a: (lambda t: t.to(torch.bfloat16) if t.is_floating_point() else t)(torch.as_tensor(a)))
    def _A_setup():
        B, S, D, H = 1, 2048, 640, 32
        return {
            "x": _w((lambda *s: torch.randn(*s, dtype=torch.bfloat16) * 0.2)(B, S, D)),
            "wqkv": _w((lambda *s: torch.randn(*s, dtype=torch.bfloat16) * 0.2)(D, 3 * D)),
            "wo": _w((lambda *s: torch.randn(*s, dtype=torch.bfloat16) * 0.05)(D, D)),
        }
    _A_in = _A_setup()

    def _B_setup():
        B, S, D, H = 1, 2048, 640, 32
        return {
            "x": _w((lambda *s: torch.randn(*s, dtype=torch.bfloat16) * 0.2)(B, S, D)),
            "wqkv": _w((lambda *s: torch.randn(*s, dtype=torch.bfloat16) * 0.2)(D, 3 * D)),
            "wo": _w((lambda *s: torch.randn(*s, dtype=torch.bfloat16) * 0.05)(D, D)),
        }
    _B_in = _B_setup()

    return {
        "A_x": _A_in["x"],
        "A_wqkv": _A_in["wqkv"],
        "A_wo": _A_in["wo"],
        "B_x": _B_in["x"],
        "B_wqkv": _B_in["wqkv"],
        "B_wo": _B_in["wo"],
    }

def reference(A_x, A_wqkv, A_wo, B_x, B_wqkv, B_wo):
    def _A_ref(x, wqkv, wo):
        B, S, D = x.shape
        H = 32
        Dh = D // H
        qkv = (lambda x, *d: x.permute(*d))((x @ wqkv).reshape(B, S, 3, H, Dh), 2, 0, 3, 1, 4)
        q, k, v = qkv[0], qkv[1], qkv[2]
        scores = torch.einsum("bhqd,bhkd->bhqk", q, k) * (Dh ** -0.5)
        p = _softmax(scores, axis=-1)
        o = torch.einsum("bhqk,bhkd->bhqd", p, v)
        return (lambda x, *d: x.permute(*d))(o, 0, 2, 1, 3).reshape(B, S, D) @ wo

    def _B_ref(x, wqkv, wo):
        B, S, D = x.shape
        H = 32
        Dh = D // H
        qkv = (lambda x, *d: x.permute(*d))((x @ wqkv).reshape(B, S, 3, H, Dh), 2, 0, 3, 1, 4)
        q, k, v = qkv[0], qkv[1], qkv[2]
        scores = torch.einsum("bhqd,bhkd->bhqk", q, k) * (Dh ** -0.5)
        p = _softmax(scores, axis=-1)
        o = torch.einsum("bhqk,bhkd->bhqd", p, v)
        return (lambda x, *d: x.permute(*d))(o, 0, 2, 1, 3).reshape(B, S, D) @ wo

    a = _A_ref(A_x, A_wqkv, A_wo)
    b = _B_ref(B_x, B_wqkv, B_wo)
    return torch.concatenate([a, b], axis=-1)
```

```cpp
#include <hip/hip_runtime.h>
#include <hip/hip_bf16.h>
#include <stdint.h>

namespace {

typedef float    f32x16 __attribute__((ext_vector_type(16)));
typedef float    f32x4  __attribute__((ext_vector_type(4)));
typedef float    f32x2  __attribute__((ext_vector_type(2)));
typedef __bf16   bf16x8 __attribute__((ext_vector_type(8)));
typedef __bf16   bf16x2 __attribute__((ext_vector_type(2)));
typedef short    s16x4  __attribute__((ext_vector_type(4)));
typedef short    s16x8  __attribute__((ext_vector_type(8)));
typedef unsigned u32x4  __attribute__((ext_vector_type(4)));
typedef unsigned u32x2  __attribute__((ext_vector_type(2)));
#define LDSP(T) __attribute__((address_space(3))) T*
#define DI __device__ __forceinline__
#define GAP() __builtin_amdgcn_sched_barrier(0)

constexpr int SEQ = 2048;
constexpr int TILE_B = 2816;
constexpr int KV_HEAD_B = 64 * TILE_B;
constexpr int Q_TILE_B = 1280;
constexpr int Q_HEAD_B = 64 * Q_TILE_B;
constexpr size_t WS_KV = 0;
constexpr size_t WS_Q = WS_KV + 64ull * KV_HEAD_B;
constexpr size_t WS_O = WS_Q + 64ull * Q_HEAD_B;
constexpr int O_PROB_B = 128 * 20 * 1024;
constexpr float QSCALE = 0.32259643f;

DI unsigned rfl(unsigned x) { return __builtin_amdgcn_readfirstlane(x); }
DI unsigned cvtpk(float lo, float hi) { f32x2 v = {lo, hi}; bf16x2 b = __builtin_convertvector(v, bf16x2); return __builtin_bit_cast(unsigned, b); }
DI __amdgpu_buffer_rsrc_t mkrsrc(const void* p, unsigned bytes) { return __builtin_amdgcn_make_buffer_rsrc((void*)p, 0, (int)bytes, 0x00020000); }
DI void dma16(unsigned m0v, unsigned voff, __amdgpu_buffer_rsrc_t r, unsigned soff) {
  asm volatile("s_mov_b32 m0, %0\n\ts_nop 0\n\tbuffer_load_dwordx4 %1, %2, %3 offen lds" :: "s"(m0v), "v"(voff), "s"(r), "s"(soff) : "memory");
}
#define WAIT_VM(n) asm volatile("s_waitcnt vmcnt(" #n ")" ::: "memory")
#define WAIT_LGKM0() asm volatile("s_waitcnt lgkmcnt(0)" ::: "memory")
#define BAR() asm volatile("s_barrier" ::: "memory")
DI f32x16 mfma16(bf16x8 a, bf16x8 b, f32x16 c) { return __builtin_amdgcn_mfma_f32_32x32x16_bf16(a, b, c, 0, 0, 0); }
DI f32x16 mfma8(s16x4 a, s16x4 b, f32x16 c) { return __builtin_amdgcn_mfma_f32_32x32x8bf16_1k(a, b, c, 0, 0, 0); }
DI f32x4 mfma16x16(bf16x8 a, bf16x8 b, f32x4 c) { return __builtin_amdgcn_mfma_f32_16x16x32_bf16(a, b, c, 0, 0, 0); }
DI s16x4 trread(unsigned a) { return __builtin_amdgcn_ds_read_tr16_b64_v4i16((LDSP(s16x4))a); }
DI bf16x8 cat8(s16x4 lo, s16x4 hi) { s16x8 v = __builtin_shufflevector(lo, hi, 0, 1, 2, 3, 4, 5, 6, 7); return __builtin_bit_cast(bf16x8, v); }
DI bf16x8 ld128(unsigned a) { return *(LDSP(bf16x8))a; }
DI s16x4 ld64(unsigned a) { return *(LDSP(s16x4))a; }

DI void decode(unsigned bid, int& p, int& hp, int& e) {
  const unsigned x = bid & 7u, s = bid >> 3;
  p = x >> 2; hp = (x & 3u) * 4 + (s & 3u); e = s >> 2;
}

constexpr int P1_STAGE = 49152, P1_W = 32768;
DI void p1_issue(unsigned lb, int st, int slot, unsigned w, unsigned vx, unsigned vw, __amdgpu_buffer_rsrc_t rx, __amdgpu_buffer_rsrc_t rw, unsigned t0) {
  const unsigned base = lb + slot * P1_STAGE + w * 1024;
#pragma unroll
  for (int i = 0; i < 4; ++i) dma16(base + i * 8192, vx, rx, t0 * 1280u + i * 81920u + st * 128u);
#pragma unroll
  for (int i = 0; i < 2; ++i) dma16(base + P1_W + i * 8192, vw, rw, (unsigned)(st * 64 + i * 32) * 3840u);
}

__global__ __launch_bounds__(512, 2) void k_qkv(const char* __restrict__ xA, const char* __restrict__ wA, const char* __restrict__ xB,
                                                const char* __restrict__ wB, char* __restrict__ ws) {
  extern __shared__ __attribute__((aligned(16))) char lds[];
  const unsigned lb = (unsigned)(size_t)(LDSP(char))lds;
  const unsigned l = threadIdx.x & 63u, w = rfl(threadIdx.x >> 6), h = l >> 5;
  int p, hp, e; decode(blockIdx.x, p, hp, e);
  const __amdgpu_buffer_rsrc_t rx = mkrsrc(p ? xB : xA, SEQ * 1280), rw = mkrsrc(p ? wB : wA, 640 * 3840);
  const unsigned t0 = e * 256;
  const unsigned vx = (8 * w + (l >> 3)) * 1280u + (((l & 7u) ^ ((4u * (w & 1u) + (l >> 4)) & 7u)) * 16u);
  unsigned vw;
  { const unsigned c0 = (l & 15u) ^ ((l >> 4) << 2), c = c0 > 14u ? 14u : c0, sec = c / 5u, sub = c - sec * 5u;
    vw = (4 * w + (l >> 4)) * 3840u + sec * 1280u + hp * 80u + sub * 16u; }
  p1_issue(lb, 0, 0, w, vx, vw, rx, rw, t0);
  p1_issue(lb, 1, 1, w, vx, vw, rx, rw, t0);
  const unsigned tq = w & 3u, hd = w >> 2;
  unsigned ax[4];
#pragma unroll
  for (int ks = 0; ks < 4; ++ks) ax[ks] = lb + (64 * tq + (l & 31u)) * 128u + (((2u * ks + h) ^ ((l >> 1) & 7u)) * 16u);
  unsigned aw[2];
  { const unsigned g = l >> 4, q = (l >> 2) & 3u, G = 4u * (g & 1u) + (l & 3u);
    const unsigned s0 = G < 5u ? G : (G == 5u ? 24u : (G == 6u ? 20u : 22u));
    const unsigned s1 = G < 5u ? 10u + G : (G == 5u ? 14u : (G == 6u ? 21u : 23u));
    const unsigned rowo = lb + P1_W + (8u * (g >> 1) + q) * 256u;
    aw[0] = rowo + (((s0 + 5u * hd) ^ (q << 3)) * 8u);
    aw[1] = rowo + (((s1 + 5u * hd) ^ (q << 3)) * 8u); }
  f32x16 acc[2][2] = {};
  for (int st = 0; st < 10; ++st) {
    const int slot = st % 3;
    if (st < 9) WAIT_VM(6); else WAIT_VM(0);
    BAR();
    if (st < 8) p1_issue(lb, st + 2, (st + 2) % 3, w, vx, vw, rx, rw, t0);
    const unsigned so = slot * P1_STAGE;
#pragma unroll
    for (int ks = 0; ks < 4; ++ks) {
      const bf16x8 x0 = ld128(ax[ks] + so), x1 = ld128(ax[ks] + so + 4096);
      const bf16x8 w0 = cat8(trread(aw[0] + so + ks * 4096), trread(aw[0] + so + ks * 4096 + 1024));
      const bf16x8 w1 = cat8(trread(aw[1] + so + ks * 4096), trread(aw[1] + so + ks * 4096 + 1024));
      acc[0][0] = mfma16(w0, x0, acc[0][0]);
      acc[0][1] = mfma16(w0, x1, acc[0][1]);
      acc[1][0] = mfma16(w1, x0, acc[1][0]);
      acc[1][1] = mfma16(w1, x1, acc[1][1]);
    }
  }
  WAIT_LGKM0();
  BAR();
  char* qg = ws + WS_Q + (size_t)(p * 32 + hp * 2 + hd) * Q_HEAD_B;
#pragma unroll
  for (int tt = 0; tt < 2; ++tt) {
    const unsigned tile = 2 * tq + tt;
    const unsigned img = lb + (hd * 8 + tile) * TILE_B, key = l & 31u;
    const f32x16 a = acc[0][tt], b = acc[1][tt];
    u32x4 q1 = {cvtpk(a[0] * QSCALE, a[1] * QSCALE), cvtpk(a[2] * QSCALE, a[3] * QSCALE), cvtpk(a[4] * QSCALE, a[5] * QSCALE), cvtpk(a[6] * QSCALE, a[7] * QSCALE)};
    char* qt = qg + (size_t)(e * 8 + tile) * Q_TILE_B;
    *(u32x4*)(qt + l * 16) = q1;
    if (h == 0) {
      u32x2 q2 = {cvtpk(a[8] * QSCALE, a[9] * QSCALE), cvtpk(a[10] * QSCALE, a[11] * QSCALE)};
      *(u32x2*)(qt + 1024 + key * 8) = q2;
      u32x2 k2 = {cvtpk(b[8], b[9]), cvtpk(b[10], b[11])};
      *(LDSP(u32x2))(img + 1024 + key * 8) = k2;
    } else {
      u32x4 v4 = {cvtpk(a[8], a[9]), cvtpk(a[10], a[11]), 0x00003F80u, 0u};
      *(LDSP(u32x4))(img + 1280 + key * 48 + 32) = v4;
    }
    u32x4 k1 = {cvtpk(b[0], b[1]), cvtpk(b[2], b[3]), cvtpk(b[4], b[5]), cvtpk(b[6], b[7])};
    *(LDSP(u32x4))(img + l * 16) = k1;
    u32x4 vv = {cvtpk(a[12], a[13]), cvtpk(a[14], a[15]), cvtpk(b[12], b[13]), cvtpk(b[14], b[15])};
    *(LDSP(u32x4))(img + 1280 + key * 48 + h * 16) = vv;
  }
  WAIT_LGKM0();
  BAR();
  for (unsigned i = threadIdx.x; i < 2 * 1408; i += 512) {
    const unsigned hh = i >= 1408u, j = i - hh * 1408u;
    const u32x4 v = *(LDSP(u32x4))(lb + i * 16);
    *(u32x4*)(ws + WS_KV + (size_t)(p * 32 + hp * 2 + hh) * KV_HEAD_B + e * 8 * TILE_B + j * 16) = v;
  }
}

constexpr int CH_HEAD_B = 4 * TILE_B;
constexpr int CH_B = 2 * CH_HEAD_B;

struct KF { bf16x8 k1; s16x4 k2; };
struct VF { bf16x8 v0, v1; };
DI KF rdK(unsigned a1, unsigned a2, int off) { KF k; k.k1 = ld128(a1 + off); k.k2 = ld64(a2 + off); return k; }
DI VF rdV(unsigned av, int off) {
  VF v;
  v.v0 = cat8(trread(av + off + 1280), trread(av + off + 1280 + 384));
  v.v1 = cat8(trread(av + off + 1280 + 768), trread(av + off + 1280 + 1152));
  return v;
}
DI bf16x8 pk8(const f32x16& s, int b) {
  u32x4 r = {cvtpk(s[b], s[b + 1]), cvtpk(s[b + 2], s[b + 3]), cvtpk(s[b + 4], s[b + 5]), cvtpk(s[b + 6], s[b + 7])};
  return __builtin_bit_cast(bf16x8, r);
}
DI float ex2(float x) { return __builtin_amdgcn_exp2f(x); }
DI float halfmax(float m) {
  auto pr = __builtin_amdgcn_permlane32_swap(__float_as_uint(m), __float_as_uint(m), false, false);
  return fmaxf(__uint_as_float(pr[0]), __uint_as_float(pr[1]));
}

DI void p2_issue(unsigned lb, int c, unsigned w, unsigned l, __amdgpu_buffer_rsrc_t rkv) {
  const unsigned hd = w >> 2, wi = w & 3u;
#pragma unroll
  for (int i = 0; i < 3; ++i) {
    unsigned pc = wi * 3 + i; pc = pc > 10u ? 10u : pc;
    dma16(lb + (c & 3) * CH_B + hd * CH_HEAD_B + pc * 1024u, l * 16u, rkv, hd * KV_HEAD_B + c * CH_HEAD_B + pc * 1024u);
  }
}

DI void phase(f32x16& sx, bf16x8& px0, bf16x8& px1, f32x16& sy, const KF& k, const bf16x8& qy1, const s16x4& qy2,
              f32x16& oy, const VF& v, const bf16x8& py0, const bf16x8& py1) {
  const f32x16 z = {};
  u32x4 r0, r1;
  sy = mfma16(k.k1, qy1, z);
  GAP();
  sx[0] = ex2(sx[0]); sx[1] = ex2(sx[1]); sx[2] = ex2(sx[2]); sx[3] = ex2(sx[3]);
  r0[0] = cvtpk(sx[0], sx[1]); r0[1] = cvtpk(sx[2], sx[3]);
  GAP();
  sy = mfma8(k.k2, qy2, sy);
  GAP();
  sx[4] = ex2(sx[4]); sx[5] = ex2(sx[5]); sx[6] = ex2(sx[6]); sx[7] = ex2(sx[7]);
  r0[2] = cvtpk(sx[4], sx[5]); r0[3] = cvtpk(sx[6], sx[7]);
  GAP();
  oy = mfma16(v.v0, py0, oy);
  GAP();
  sx[8] = ex2(sx[8]); sx[9] = ex2(sx[9]); sx[10] = ex2(sx[10]); sx[11] = ex2(sx[11]);
  r1[0] = cvtpk(sx[8], sx[9]); r1[1] = cvtpk(sx[10], sx[11]);
  GAP();
  oy = mfma16(v.v1, py1, oy);
  GAP();
  sx[12] = ex2(sx[12]); sx[13] = ex2(sx[13]); sx[14] = ex2(sx[14]); sx[15] = ex2(sx[15]);
  r1[2] = cvtpk(sx[12], sx[13]); r1[3] = cvtpk(sx[14], sx[15]);
  GAP();
  px0 = __builtin_bit_cast(bf16x8, r0); px1 = __builtin_bit_cast(bf16x8, r1);
}

DI void attn_fast(unsigned lb, unsigned w, unsigned l, __amdgpu_buffer_rsrc_t rkv, unsigned a1, unsigned a2, unsigned av,
                  const bf16x8& qa1, s16x4 qa2, const bf16x8& qb1, s16x4 qb2, f32x16& oa, f32x16& ob) {
  const unsigned h = l >> 5;
  const f32x16 z = {};
  p2_issue(lb, 0, w, l, rkv); p2_issue(lb, 1, w, l, rkv); p2_issue(lb, 2, w, l, rkv);
  WAIT_VM(3); BAR();
  p2_issue(lb, 3, w, l, rkv);
  KF k = rdK(a1, a2, 0);
  f32x16 sa = mfma8(k.k2, qa2, mfma16(k.k1, qa1, z)), sb = mfma8(k.k2, qb2, mfma16(k.k1, qb1, z));
  float ma = sa[0], mb = sb[0];
#pragma unroll
  for (int i = 1; i < 16; ++i) { ma = fmaxf(ma, sa[i]); mb = fmaxf(mb, sb[i]); }
  ma = halfmax(ma); mb = halfmax(mb);
  if (h) { qa2[0] = (short)(cvtpk(-ma, 0.f) & 0xffffu); qb2[0] = (short)(cvtpk(-mb, 0.f) & 0xffffu); }
  sa = mfma8(k.k2, qa2, mfma16(k.k1, qa1, z));
  oa = z; ob = z;
  bf16x8 pa0, pa1, pb0 = {}, pb1 = {};
  VF vp = rdV(av, 0);
  for (int c = 0; c < 16; ++c) {
    if (c > 0) {
      if (c < 14) WAIT_VM(3); else WAIT_VM(0);
      WAIT_LGKM0();
      BAR();
      if (c < 13) p2_issue(lb, c + 3, w, l, rkv);
    }
    const unsigned so = (c & 3) * CH_B, sn = ((c + 1) & 3) * CH_B;
#pragma unroll
    for (int j = 0; j < 4; ++j) {
      VF v = rdV(av + so, j * TILE_B);
      KF kn = j < 3 ? rdK(a1 + so, a2 + so, (j + 1) * TILE_B) : rdK(a1 + sn, a2 + sn, 0);
      phase(sa, pa0, pa1, sb, k, qb1, qb2, ob, vp, pb0, pb1);
      phase(sb, pb0, pb1, sa, kn, qa1, qa2, oa, v, pa0, pa1);
      k = kn; vp = v;
    }
  }
  ob = mfma16(vp.v0, pb0, ob); ob = mfma16(vp.v1, pb1, ob);
}

DI void attn_safe(unsigned lb, unsigned w, unsigned l, __amdgpu_buffer_rsrc_t rkv, unsigned a1, unsigned a2, unsigned av,
                  const bf16x8& qa1, s16x4 qa2, const bf16x8& qb1, s16x4 qb2, f32x16& oa, f32x16& ob) {
  const unsigned h = l >> 5;
  const f32x16 z = {};
  if (h) { qa2[0] = 0; qb2[0] = 0; }
  oa = z; ob = z;
  float ma = -3.0e38f, mb = -3.0e38f;
  for (int c = 0; c < 16; ++c) {
    WAIT_LGKM0(); BAR();
    p2_issue(lb, c, w, l, rkv);
    WAIT_VM(0); BAR();
    const unsigned so = (c & 3) * CH_B;
    for (int j = 0; j < 4; ++j) {
      const KF k = rdK(a1 + so + j * TILE_B, a2 + so + j * TILE_B, 0);
      const VF v = rdV(av + so + j * TILE_B, 0);
      f32x16 sa = mfma8(k.k2, qa2, mfma16(k.k1, qa1, z)), sb = mfma8(k.k2, qb2, mfma16(k.k1, qb1, z));
      float xa = ma, xb = mb;
#pragma unroll
      for (int i = 0; i < 16; ++i) { xa = fmaxf(xa, sa[i]); xb = fmaxf(xb, sb[i]); }
      xa = halfmax(xa); xb = halfmax(xb);
      const float fa = ex2(ma - xa), fb = ex2(mb - xb);
      ma = xa; mb = xb;
#pragma unroll
      for (int i = 0; i < 16; ++i) { sa[i] = ex2(sa[i] - xa); sb[i] = ex2(sb[i] - xb); oa[i] *= fa; ob[i] *= fb; }
      oa = mfma16(v.v0, pk8(sa, 0), oa); oa = mfma16(v.v1, pk8(sa, 8), oa);
      ob = mfma16(v.v0, pk8(sb, 0), ob); ob = mfma16(v.v1, pk8(sb, 8), ob);
    }
  }
}

DI float rowsum_of(const f32x16& o) {
  auto pr = __builtin_amdgcn_permlane32_swap(__float_as_uint(o[8]), __float_as_uint(o[8]), false, false);
  return __uint_as_float(pr[1]);
}
DI bool bad_result(const f32x16& o, float lsum, unsigned h) {
  bool bad = !(lsum >= 8.0e-25f && lsum <= 1.0e30f);
  float mx = 0.f;
#pragma unroll
  for (int i = 0; i < 8; ++i) mx = fmaxf(mx, fabsf(o[i]));
  if (h == 0) {
#pragma unroll
    for (int i = 8; i < 12; ++i) mx = fmaxf(mx, fabsf(o[i]));
  }
  float sm = 0.f;
#pragma unroll
  for (int i = 0; i < 8; ++i) sm += o[i] * 0.f;
  if (h == 0) {
#pragma unroll
    for (int i = 8; i < 12; ++i) sm += o[i] * 0.f;
  }
  return bad || !(mx <= 3.0e38f) || !(sm == 0.f);
}

DI bool wg_any(unsigned a, unsigned w, unsigned l, bool v) {
  const unsigned any = __any((int)v) ? 1u : 0u;
  if (l == 0) *(LDSP(unsigned))(a + w * 4) = any;
  WAIT_LGKM0(); BAR();
  const unsigned f = *(LDSP(unsigned))(a + (l & 7u) * 4);
  return __any((int)f) != 0;
}

__global__ __launch_bounds__(512, 2) void k_attn(char* __restrict__ ws) {
  extern __shared__ __attribute__((aligned(16))) char lds[];
  const unsigned lb = (unsigned)(size_t)(LDSP(char))lds;
  const unsigned l = threadIdx.x & 63u, w = rfl(threadIdx.x >> 6), h = l >> 5;
  int p, hp, e; decode(blockIdx.x, p, hp, e);
  const unsigned hd = w >> 2, tq = w & 3u, head = hp * 2 + hd;
  const __amdgpu_buffer_rsrc_t rkv = mkrsrc(ws + WS_KV + (size_t)(p * 32 + hp * 2) * KV_HEAD_B, 2 * KV_HEAD_B);
  const char* qg = ws + WS_Q + (size_t)(p * 32 + head) * Q_HEAD_B + (size_t)(e * 8 + tq * 2) * Q_TILE_B;
  const bf16x8 qa1 = *(const bf16x8*)(qg + l * 16), qb1 = *(const bf16x8*)(qg + Q_TILE_B + l * 16);
  s16x4 qa2 = *(const s16x4*)(qg + 1024 + (l & 31u) * 8), qb2 = *(const s16x4*)(qg + Q_TILE_B + 1024 + (l & 31u) * 8);
  if (h) { qa2 = s16x4{0, 0, 0, 0}; qb2 = s16x4{0, 0, 0, 0}; }
  const unsigned hb = lb + hd * CH_HEAD_B;
  const unsigned a1 = hb + l * 16;
  const unsigned a2 = hb + (h ? 1280u + 40u : 1024u + (l & 31u) * 8u);
  const unsigned av = hb + (4u * h + ((l >> 2) & 3u)) * 48u + ((l >> 4) & 1u) * 32u + (l & 3u) * 8u;
  f32x16 oa, ob;
  attn_fast(lb, w, l, rkv, a1, a2, av, qa1, qa2, qb1, qb2, oa, ob);
  float la = rowsum_of(oa), lbs = rowsum_of(ob);
  if (wg_any(lb + 4 * CH_B, w, l, bad_result(oa, la, h) || bad_result(ob, lbs, h))) {
    attn_safe(lb, w, l, rkv, a1, a2, av, qa1, qa2, qb1, qb2, oa, ob);
    la = rowsum_of(oa); lbs = rowsum_of(ob);
  }
  char* og = ws + WS_O + (size_t)p * O_PROB_B;
#pragma unroll
  for (int tt = 0; tt < 2; ++tt) {
    const f32x16& o = tt ? ob : oa;
    const float r = 1.0f / (tt ? lbs : la);
    const unsigned tok = e * 256 + tq * 64 + tt * 32 + (l & 31u);
#pragma unroll
    for (int g = 0; g < 3; ++g) {
      if (g < 2 || h == 0) {
        const unsigned k = head * 20 + (g * 2 + h) * 4;
        u32x2 v = {cvtpk(o[4 * g] * r, o[4 * g + 1] * r), cvtpk(o[4 * g + 2] * r, o[4 * g + 3] * r)};
        *(u32x2*)(og + (tok >> 4) * 20480u + (k >> 5) * 1024u + (((k >> 3) & 3u) * 16u + (tok & 15u)) * 16u + ((k >> 2) & 1u) * 8u) = v;
      }
    }
  }
}

__global__ __launch_bounds__(512, 2) void k_out(const char* __restrict__ woA, const char* __restrict__ woB, const char* __restrict__ ws,
                                                char* __restrict__ out) {
  extern __shared__ __attribute__((aligned(16))) char lds[];
  const unsigned lb = (unsigned)(size_t)(LDSP(char))lds;
  const unsigned l = threadIdx.x & 63u, w = rfl(threadIdx.x >> 6);
  int p, hp, e; decode(blockIdx.x, p, hp, e);
  const unsigned cb = hp >> 1, th = hp & 1;
  const __amdgpu_buffer_rsrc_t rw = mkrsrc(p ? woB : woA, 640 * 1280);
  for (unsigned i = w; i < 100; i += 8) {
    const unsigned pc = i * 64 + l, r = pc / 10u, c = pc - r * 10u;
    const unsigned k = (r & ~12u) | ((r & 4u) << 1) | ((r & 8u) >> 1);
    dma16(lb + i * 1024, k * 1280u + cb * 160u + c * 16u, rw, 0u);
  }
  const unsigned t16 = e * 16 + th * 8 + w;
  const char* og = ws + WS_O + (size_t)p * O_PROB_B + (size_t)t16 * 20480 + l * 16;
  bf16x8 of[20];
#pragma unroll
  for (int s = 0; s < 20; ++s) of[s] = *(const bf16x8*)(og + s * 1024);
  WAIT_VM(0); BAR();
  const unsigned g = l >> 4, q = (l >> 2) & 3u;
  const unsigned aw = lb + (16u * (g >> 1) + 4u * (g & 1u) + q) * 160u + (l & 3u) * 8u;
  f32x4 acc[5] = {};
#pragma unroll
  for (int s = 0; s < 20; ++s)
#pragma unroll
    for (int t = 0; t < 5; ++t) {
      const bf16x8 a = cat8(trread(aw + s * 5120 + t * 32), trread(aw + s * 5120 + t * 32 + 8 * 160));
      acc[t] = mfma16x16(a, of[s], acc[t]);
    }
  const unsigned tok = t16 * 16 + (l & 15u);
  char* orow = out + (size_t)tok * 2560 + p * 1280 + cb * 160 + g * 8;
#pragma unroll
  for (int t = 0; t < 5; ++t) {
    u32x2 v = {cvtpk(acc[t][0], acc[t][1]), cvtpk(acc[t][2], acc[t][3])};
    *(u32x2*)(orow + t * 32) = v;
  }
}

}

extern "C" void kernel_launch(void* const* d_in, const int* in_sizes, int n_in, void* d_out, int out_size, void* d_ws, size_t ws_size,
                              hipStream_t stream) {
  const char* xA = (const char*)d_in[0]; const char* wA = (const char*)d_in[1]; const char* woA = (const char*)d_in[2];
  const char* xB = (const char*)d_in[3]; const char* wB = (const char*)d_in[4]; const char* woB = (const char*)d_in[5];
  char* ws = (char*)d_ws;
  (void)hipFuncSetAttribute((const void*)k_qkv, hipFuncAttributeMaxDynamicSharedMemorySize, 3 * P1_STAGE);
  (void)hipFuncSetAttribute((const void*)k_attn, hipFuncAttributeMaxDynamicSharedMemorySize, 4 * CH_B + 64);
  (void)hipFuncSetAttribute((const void*)k_out, hipFuncAttributeMaxDynamicSharedMemorySize, 102400);
  k_qkv<<<256, 512, 3 * P1_STAGE, stream>>>(xA, wA, xB, wB, ws);
  k_attn<<<256, 512, 4 * CH_B + 64, stream>>>(ws);
  k_out<<<256, 512, 102400, stream>>>(woA, woB, ws, (char*)d_out);
}
```

```cpp
#include <hip/hip_runtime.h>
#include <hip/hip_bf16.h>
#include <stdint.h>

namespace {

typedef float    f32x16 __attribute__((ext_vector_type(16)));
typedef float    f32x4  __attribute__((ext_vector_type(4)));
typedef float    f32x2  __attribute__((ext_vector_type(2)));
typedef float    f32x8  __attribute__((ext_vector_type(8)));
typedef __bf16   bf16x16 __attribute__((ext_vector_type(16)));
typedef __bf16   bf16x8 __attribute__((ext_vector_type(8)));
typedef __bf16   bf16x2 __attribute__((ext_vector_type(2)));
typedef short    s16x4  __attribute__((ext_vector_type(4)));
typedef short    s16x8  __attribute__((ext_vector_type(8)));
typedef unsigned u32x16 __attribute__((ext_vector_type(16)));
typedef unsigned u32x8  __attribute__((ext_vector_type(8)));
typedef unsigned u32x4  __attribute__((ext_vector_type(4)));
typedef unsigned u32x2  __attribute__((ext_vector_type(2)));
#define LDSP(T) __attribute__((address_space(3))) T*
#define DI __device__ __forceinline__

constexpr int SEQ = 2048;
constexpr int PAIR_B = 5376;
constexpr int CH_HEAD = 4 * PAIR_B;
constexpr int CH = 2 * CH_HEAD;
constexpr int O_PROB = 128 * 20480;
constexpr unsigned WS_KV = 0;
constexpr unsigned WS_O = 2 * 16 * 8 * CH;
constexpr unsigned FL_K = 2 * O_PROB, FL_O = FL_K + 4096, FL_E = FL_O + 4096, OF_BYTES = FL_E + 4096;
constexpr unsigned EMPTY = 0xFFFFFFFFu;
constexpr int P1_STAGE = 49152;
constexpr int LDS_BYTES = 3 * P1_STAGE + 256;
constexpr float QSCALE = 0.32259643f;
constexpr int SC1 = 16;
constexpr unsigned SPIN_LIMIT = 1u << 21;

DI unsigned rfl(unsigned x) { return __builtin_amdgcn_readfirstlane(x); }
DI unsigned cvtpk(float lo, float hi) { f32x2 v = {lo, hi}; bf16x2 b = __builtin_convertvector(v, bf16x2); return __builtin_bit_cast(unsigned, b); }
DI __amdgpu_buffer_rsrc_t mkrsrc(const void* p, unsigned bytes) { return __builtin_amdgcn_make_buffer_rsrc((void*)p, 0, (int)bytes, 0x00020000); }
DI void dma16(unsigned m0v, unsigned voff, __amdgpu_buffer_rsrc_t r, unsigned soff) {
  asm volatile("s_mov_b32 m0, %0\n\ts_nop 0\n\tbuffer_load_dwordx4 %1, %2, %3 offen lds" :: "s"(m0v), "v"(voff), "s"(r), "s"(soff) : "memory");
}
DI void dma4(unsigned m0v, unsigned voff, __amdgpu_buffer_rsrc_t r, unsigned soff) {
  asm volatile("s_mov_b32 m0, %0\n\ts_nop 0\n\tbuffer_load_dword %1, %2, %3 offen lds" :: "s"(m0v), "v"(voff), "s"(r), "s"(soff) : "memory");
}
#define WAIT_VM0() asm volatile("s_waitcnt vmcnt(0)" ::: "memory")
#define WAIT_LGKM0() asm volatile("s_waitcnt lgkmcnt(0)" ::: "memory")
#define BAR() asm volatile("s_barrier" ::: "memory")
DI f32x16 mfma16(bf16x8 a, bf16x8 b, f32x16 c) { return __builtin_amdgcn_mfma_f32_32x32x16_bf16(a, b, c, 0, 0, 0); }
DI f32x16 mfma8(s16x4 a, s16x4 b, f32x16 c) { return __builtin_amdgcn_mfma_f32_32x32x8bf16_1k(a, b, c, 0, 0, 0); }
DI f32x4 mfma16x16(bf16x8 a, bf16x8 b, f32x4 c) { return __builtin_amdgcn_mfma_f32_16x16x32_bf16(a, b, c, 0, 0, 0); }
DI bf16x8 as8(u32x4 v) { return __builtin_bit_cast(bf16x8, v); }
DI s16x4 as4(u32x2 v) { return __builtin_bit_cast(s16x4, v); }
DI s16x4 trread(unsigned a) { return __builtin_amdgcn_ds_read_tr16_b64_v4i16((LDSP(s16x4))(size_t)a); }
DI bf16x8 cat8(s16x4 lo, s16x4 hi) { s16x8 v = __builtin_shufflevector(lo, hi, 0, 1, 2, 3, 4, 5, 6, 7); return __builtin_bit_cast(bf16x8, v); }
DI u32x4 lds_ld4(unsigned a) { return *(LDSP(u32x4))(size_t)a; }
DI void lds_st4(unsigned a, u32x4 v) { *(LDSP(u32x4))(size_t)a = v; }
DI u32x4 gld4(__amdgpu_buffer_rsrc_t r, unsigned vo, unsigned so) { return __builtin_amdgcn_raw_buffer_load_b128(r, vo, so, SC1); }
DI u32x2 gld2(__amdgpu_buffer_rsrc_t r, unsigned vo, unsigned so) { return __builtin_amdgcn_raw_buffer_load_b64(r, vo, so, SC1); }
DI void gst4(u32x4 v, __amdgpu_buffer_rsrc_t r, unsigned vo, unsigned so) { __builtin_amdgcn_raw_buffer_store_b128(v, r, vo, so, SC1); }
DI void gst2(u32x2 v, __amdgpu_buffer_rsrc_t r, unsigned vo, unsigned so) { __builtin_amdgcn_raw_buffer_store_b64(v, r, vo, so, SC1); }
DI float ex2(float x) { return __builtin_amdgcn_exp2f(x); }
DI float halfmax(float m) {
  auto pr = __builtin_amdgcn_permlane32_swap(__float_as_uint(m), __float_as_uint(m), false, false);
  return fmaxf(__uint_as_float(pr[0]), __uint_as_float(pr[1]));
}
DI f32x16 spv(u32x4 v, const f32x16& s, int b, f32x16 o) {
  const f32x8 t = {s[b], s[b + 1], s[b + 2], s[b + 3], s[b + 4], s[b + 5], s[b + 6], s[b + 7]};
  return __builtin_amdgcn_smfmac_f32_32x32x32_bf16(as8(v), __builtin_bit_cast(bf16x16, t), o, (int)0xDDDDDDDD, 0, 0);
}
DI void swap16(u32x2& a, u32x2& b) {
  auto r0 = __builtin_amdgcn_permlane16_swap(a[0], b[0], false, false);
  auto r1 = __builtin_amdgcn_permlane16_swap(a[1], b[1], false, false);
  a = u32x2{r0[0], r1[0]}; b = u32x2{r0[1], r1[1]};
}
DI void swap32(u32x2& a, u32x2& b) {
  auto r0 = __builtin_amdgcn_permlane32_swap(a[0], b[0], false, false);
  auto r1 = __builtin_amdgcn_permlane32_swap(a[1], b[1], false, false);
  a = u32x2{r0[0], r1[0]}; b = u32x2{r0[1], r1[1]};
}
DI u32x4 cat(u32x2 a, u32x2 b) { return u32x4{a[0], a[1], b[0], b[1]}; }

DI void decode(unsigned bid, unsigned& p, unsigned& hp, unsigned& e, unsigned& tile3, unsigned& col3) {
  const unsigned x = bid & 7u, s = bid >> 3;
  p = x >> 2; tile3 = (x & 3u) * 4 + (s & 3u); col3 = s >> 2;
  hp = ((x >> 1) & 1u) * 8 + (s & 7u); e = (x & 1u) * 4 + (s >> 3);
}

#define MAIN_ASM \
  "s_add_u32 %[mb], %[lw], 49152\n\t" \
  "s_lshl_b32 %[tmp], %[kq], 7\n\t" \
  "s_add_u32 %[sx], %[sx0], %[tmp]\n\t" \
  "s_mul_i32 %[sw], %[kq], 245760\n\t" \
  "s_mov_b32 m0, %[mb]\n\t" \
  "s_add_u32 %[tmp], %[sx], 0\n\t" \
  "buffer_load_dwordx4 v134, %[rx], %[tmp] offen lds\n\t" \
  "s_add_u32 m0, %[mb], 8192\n\t" \
  "s_add_u32 %[tmp], %[sx], 81920\n\t" \
  "buffer_load_dwordx4 v134, %[rx], %[tmp] offen lds\n\t" \
  "s_add_u32 m0, %[mb], 16384\n\t" \
  "s_add_u32 %[tmp], %[sx], 163840\n\t" \
  "buffer_load_dwordx4 v134, %[rx], %[tmp] offen lds\n\t" \
  "s_add_u32 m0, %[mb], 24576\n\t" \
  "s_add_u32 %[tmp], %[sx], 245760\n\t" \
  "buffer_load_dwordx4 v134, %[rx], %[tmp] offen lds\n\t" \
  "s_add_u32 m0, %[mb], 32768\n\t" \
  "s_add_u32 %[tmp], %[sw], 0\n\t" \
  "buffer_load_dwordx4 v135, %[rw], %[tmp] offen lds\n\t" \
  "s_add_u32 m0, %[mb], 40960\n\t" \
  "s_add_u32 %[tmp], %[sw], 122880\n\t" \
  "buffer_load_dwordx4 v135, %[rw], %[tmp] offen lds\n\t" \
  "s_add_u32 %[kq], %[kq], 1\n\t" \
  "s_cmp_eq_u32 %[kq], 10\n\t" \
  "s_cselect_b32 %[kq], 0, %[kq]\n\t" \
  "s_add_u32 %[mb], %[lw], 98304\n\t" \
  "s_lshl_b32 %[tmp], %[kq], 7\n\t" \
  "s_add_u32 %[sx], %[sx0], %[tmp]\n\t" \
  "s_mul_i32 %[sw], %[kq], 245760\n\t" \
  "s_add_u32 %[kq], %[kq], 1\n\t" \
  "s_cmp_eq_u32 %[kq], 10\n\t" \
  "s_cselect_b32 %[kq], 0, %[kq]\n\t" \
  "s_mov_b32 %[st], 0\n\t" \
  "s_mov_b32 %[cur], 0\n\t" \
  "s_waitcnt vmcnt(6)\n\t" \
  "s_barrier\n\t" \
  "ds_read_b64_tr_b16 v[80:81], v130 offset:0\n\t" \
  "ds_read_b64_tr_b16 v[82:83], v130 offset:1024\n\t" \
  "ds_read_b128 v[64:67], v128 offset:0\n\t" \
  "ds_read_b128 v[68:71], v128 offset:2048\n\t" \
  "ds_read_b128 v[72:75], v128 offset:4096\n\t" \
  "ds_read_b128 v[76:79], v128 offset:6144\n\t" \
  "ds_read_b64_tr_b16 v[84:85], v131 offset:0\n\t" \
  "ds_read_b64_tr_b16 v[86:87], v131 offset:1024\n\t" \
  "ds_read_b64_tr_b16 v[88:89], v132 offset:0\n\t" \
  "ds_read_b64_tr_b16 v[90:91], v132 offset:1024\n\t" \
  "ds_read_b64_tr_b16 v[92:93], v133 offset:0\n\t" \
  "ds_read_b64_tr_b16 v[94:95], v133 offset:1024\n\t" \
  "s_waitcnt lgkmcnt(0)\n\t" \
  "v_mfma_f32_16x16x32_bf16 v[0:3], v[80:83], v[64:67], 0\n\t" \
  "ds_read_b64_tr_b16 v[112:113], v130 offset:8192\n\t" \
  "s_mov_b32 m0, %[mb]\n\t" \
  "s_add_u32 %[tmp], %[sx], 0\n\t" \
  "buffer_load_dwordx4 v134, %[rx], %[tmp] offen lds\n\t" \
  "v_mov_b64 v[136:137], 0\n\t" \
  "v_mov_b64 v[176:177], 0\n\t" \
  "v_mfma_f32_16x16x32_bf16 v[16:19], v[80:83], v[68:71], 0\n\t" \
  "ds_read_b64_tr_b16 v[114:115], v130 offset:9216\n\t" \
  "v_mov_b64 v[138:139], 0\n\t" \
  "v_mov_b64 v[178:179], 0\n\t" \
  "v_mfma_f32_16x16x32_bf16 v[32:35], v[80:83], v[72:75], 0\n\t" \
  "ds_read_b128 v[96:99], v129 offset:0\n\t" \
  "s_add_u32 m0, %[mb], 8192\n\t" \
  "s_add_u32 %[tmp], %[sx], 81920\n\t" \
  "buffer_load_dwordx4 v134, %[rx], %[tmp] offen lds\n\t" \
  "v_mov_b64 v[140:141], 0\n\t" \
  "v_mov_b64 v[180:181], 0\n\t" \
  "v_mfma_f32_16x16x32_bf16 v[48:51], v[80:83], v[76:79], 0\n\t" \
  "ds_read_b128 v[100:103], v129 offset:2048\n\t" \
  "v_mov_b64 v[142:143], 0\n\t" \
  "v_mov_b64 v[182:183], 0\n\t" \
  "v_mfma_f32_16x16x32_bf16 v[4:7], v[84:87], v[64:67], 0\n\t" \
  "ds_read_b128 v[104:107], v129 offset:4096\n\t" \
  "v_mov_b64 v[144:145], 0\n\t" \
  "v_mov_b64 v[220:221], 0\n\t" \
  "v_mfma_f32_16x16x32_bf16 v[20:23], v[84:87], v[68:71], 0\n\t" \
  "ds_read_b128 v[108:111], v129 offset:6144\n\t" \
  "s_add_u32 m0, %[mb], 16384\n\t" \
  "s_add_u32 %[tmp], %[sx], 163840\n\t" \
  "buffer_load_dwordx4 v134, %[rx], %[tmp] offen lds\n\t" \
  "v_mov_b64 v[146:147], 0\n\t" \
  "v_mov_b64 v[226:227], 0\n\t" \
  "v_mfma_f32_16x16x32_bf16 v[36:39], v[84:87], v[72:75], 0\n\t" \
  "ds_read_b64_tr_b16 v[116:117], v131 offset:8192\n\t" \
  "v_mov_b64 v[148:149], 0\n\t" \
  "v_mov_b32 v168, 0xdddddddd\n\t" \
  "v_mfma_f32_16x16x32_bf16 v[52:55], v[84:87], v[76:79], 0\n\t" \
  "ds_read_b64_tr_b16 v[118:119], v131 offset:9216\n\t" \
  "s_add_u32 m0, %[mb], 24576\n\t" \
  "s_add_u32 %[tmp], %[sx], 245760\n\t" \
  "buffer_load_dwordx4 v134, %[rx], %[tmp] offen lds\n\t" \
  "v_mov_b64 v[150:151], 0\n\t" \
  "v_mfma_f32_16x16x32_bf16 v[8:11], v[88:91], v[64:67], 0\n\t" \
  "ds_read_b64_tr_b16 v[120:121], v132 offset:8192\n\t" \
  "v_mov_b64 v[152:153], 0\n\t" \
  "v_mfma_f32_16x16x32_bf16 v[24:27], v[88:91], v[68:71], 0\n\t" \
  "ds_read_b64_tr_b16 v[122:123], v132 offset:9216\n\t" \
  "v_mov_b64 v[154:155], 0\n\t" \
  "v_mfma_f32_16x16x32_bf16 v[40:43], v[88:91], v[72:75], 0\n\t" \
  "ds_read_b64_tr_b16 v[124:125], v133 offset:8192\n\t" \
  "s_add_u32 m0, %[mb], 32768\n\t" \
  "s_add_u32 %[tmp], %[sw], 0\n\t" \
  "buffer_load_dwordx4 v135, %[rw], %[tmp] offen lds\n\t" \
  "v_mov_b64 v[156:157], 0\n\t" \
  "v_mfma_f32_16x16x32_bf16 v[56:59], v[88:91], v[76:79], 0\n\t" \
  "ds_read_b64_tr_b16 v[126:127], v133 offset:9216\n\t" \
  "v_mov_b64 v[158:159], 0\n\t" \
  "s_add_u32 %[nxt], %[cur], 49152\n\t" \
  "s_cmp_eq_u32 %[cur], 98304\n\t" \
  "s_cselect_b32 %[nxt], 0, %[nxt]\n\t" \
  "s_sub_u32 %[dl], %[nxt], %[cur]\n\t" \
  "v_mfma_f32_16x16x32_bf16 v[12:15], v[92:95], v[64:67], 0\n\t" \
  "s_add_u32 m0, %[mb], 40960\n\t" \
  "s_add_u32 %[tmp], %[sw], 122880\n\t" \
  "buffer_load_dwordx4 v135, %[rw], %[tmp] offen lds\n\t" \
  "v_mov_b64 v[160:161], 0\n\t" \
  "v_add_u32 v128, %[dl], v128\n\t" \
  "v_add_u32 v129, %[dl], v129\n\t" \
  "v_mfma_f32_16x16x32_bf16 v[28:31], v[92:95], v[68:71], 0\n\t" \
  "v_mov_b64 v[162:163], 0\n\t" \
  "v_add_u32 v130, %[dl], v130\n\t" \
  "v_add_u32 v131, %[dl], v131\n\t" \
  "s_add_u32 %[mb], %[lw], %[cur]\n\t" \
  "s_lshl_b32 %[tmp], %[kq], 7\n\t" \
  "s_add_u32 %[sx], %[sx0], %[tmp]\n\t" \
  "s_mul_i32 %[sw], %[kq], 245760\n\t" \
  "v_mfma_f32_16x16x32_bf16 v[44:47], v[92:95], v[72:75], 0\n\t" \
  "v_mov_b64 v[164:165], 0\n\t" \
  "v_add_u32 v132, %[dl], v132\n\t" \
  "v_add_u32 v133, %[dl], v133\n\t" \
  "v_mfma_f32_16x16x32_bf16 v[60:63], v[92:95], v[76:79], 0\n\t" \
  "v_mov_b64 v[166:167], 0\n\t" \
  "s_add_u32 %[kq], %[kq], 1\n\t" \
  "s_cmp_eq_u32 %[kq], 10\n\t" \
  "s_cselect_b32 %[kq], 0, %[kq]\n\t" \
  "s_mov_b32 %[cur], %[nxt]\n\t" \
  "s_waitcnt lgkmcnt(0)\n\t" \
  "s_waitcnt vmcnt(6)\n\t" \
  "s_branch LB_%=\n\t" \
  "LP1_%=:\n\t" \
  "s_waitcnt lgkmcnt(0)\n\t" \
  "s_cmp_lt_u32 %[st], 8\n\t" \
  "s_cbranch_scc0 LN1_%=\n\t" \
  "v_mfma_f32_16x16x32_bf16 v[0:3], v[80:83], v[64:67], v[0:3]\n\t" \
  "ds_read_b64_tr_b16 v[112:113], v130 offset:8192\n\t" \
  "v_mfma_f32_16x16x32_bf16 v[16:19], v[80:83], v[68:71], v[16:19]\n\t" \
  "ds_read_b64_tr_b16 v[114:115], v130 offset:9216\n\t" \
  "v_mfma_f32_16x16x32_bf16 v[32:35], v[80:83], v[72:75], v[32:35]\n\t" \
  "ds_read_b128 v[96:99], v129 offset:0\n\t" \
  "s_add_u32 m0, %[mb], 24576\n\t" \
  "s_add_u32 %[tmp], %[sx], 245760\n\t" \
  "buffer_load_dwordx4 v134, %[rx], %[tmp] offen lds\n\t" \
  "v_mfma_f32_16x16x32_bf16 v[48:51], v[80:83], v[76:79], v[48:51]\n\t" \
  "ds_read_b128 v[100:103], v129 offset:2048\n\t" \
  "v_mfma_f32_16x16x32_bf16 v[4:7], v[84:87], v[64:67], v[4:7]\n\t" \
  "ds_read_b128 v[104:107], v129 offset:4096\n\t" \
  "v_mfma_f32_16x16x32_bf16 v[20:23], v[84:87], v[68:71], v[20:23]\n\t" \
  "ds_read_b128 v[108:111], v129 offset:6144\n\t" \
  "v_mfma_f32_16x16x32_bf16 v[36:39], v[84:87], v[72:75], v[36:39]\n\t" \
  "ds_read_b64_tr_b16 v[116:117], v131 offset:8192\n\t" \
  "v_mfma_f32_16x16x32_bf16 v[52:55], v[84:87], v[76:79], v[52:55]\n\t" \
  "ds_read_b64_tr_b16 v[118:119], v131 offset:9216\n\t" \
  "s_add_u32 m0, %[mb], 32768\n\t" \
  "s_add_u32 %[tmp], %[sw], 0\n\t" \
  "buffer_load_dwordx4 v135, %[rw], %[tmp] offen lds\n\t" \
  "v_mfma_f32_16x16x32_bf16 v[8:11], v[88:91], v[64:67], v[8:11]\n\t" \
  "ds_read_b64_tr_b16 v[120:121], v132 offset:8192\n\t" \
  "v_mfma_f32_16x16x32_bf16 v[24:27], v[88:91], v[68:71], v[24:27]\n\t" \
  "ds_read_b64_tr_b16 v[122:123], v132 offset:9216\n\t" \
  "v_mfma_f32_16x16x32_bf16 v[40:43], v[88:91], v[72:75], v[40:43]\n\t" \
  "ds_read_b64_tr_b16 v[124:125], v133 offset:8192\n\t" \
  "v_mfma_f32_16x16x32_bf16 v[56:59], v[88:91], v[76:79], v[56:59]\n\t" \
  "ds_read_b64_tr_b16 v[126:127], v133 offset:9216\n\t" \
  "s_add_u32 %[nxt], %[cur], 49152\n\t" \
  "s_cmp_eq_u32 %[cur], 98304\n\t" \
  "s_cselect_b32 %[nxt], 0, %[nxt]\n\t" \
  "s_sub_u32 %[dl], %[nxt], %[cur]\n\t" \
  "v_mfma_f32_16x16x32_bf16 v[12:15], v[92:95], v[64:67], v[12:15]\n\t" \
  "s_add_u32 m0, %[mb], 40960\n\t" \
  "s_add_u32 %[tmp], %[sw], 122880\n\t" \
  "buffer_load_dwordx4 v135, %[rw], %[tmp] offen lds\n\t" \
  "v_add_u32 v128, %[dl], v128\n\t" \
  "v_add_u32 v129, %[dl], v129\n\t" \
  "v_mfma_f32_16x16x32_bf16 v[28:31], v[92:95], v[68:71], v[28:31]\n\t" \
  "v_add_u32 v130, %[dl], v130\n\t" \
  "v_add_u32 v131, %[dl], v131\n\t" \
  "s_add_u32 %[mb], %[lw], %[cur]\n\t" \
  "s_lshl_b32 %[tmp], %[kq], 7\n\t" \
  "s_add_u32 %[sx], %[sx0], %[tmp]\n\t" \
  "s_mul_i32 %[sw], %[kq], 245760\n\t" \
  "v_mfma_f32_16x16x32_bf16 v[44:47], v[92:95], v[72:75], v[44:47]\n\t" \
  "v_add_u32 v132, %[dl], v132\n\t" \
  "v_add_u32 v133, %[dl], v133\n\t" \
  "v_mfma_f32_16x16x32_bf16 v[60:63], v[92:95], v[76:79], v[60:63]\n\t" \
  "s_add_u32 %[kq], %[kq], 1\n\t" \
  "s_cmp_eq_u32 %[kq], 10\n\t" \
  "s_cselect_b32 %[kq], 0, %[kq]\n\t" \
  "s_mov_b32 %[cur], %[nxt]\n\t" \
  "s_waitcnt lgkmcnt(0)\n\t" \
  "s_waitcnt vmcnt(6)\n\t" \
  "s_branch LB_%=\n\t" \
  "LN1_%=:\n\t" \
  "v_mfma_f32_16x16x32_bf16 v[0:3], v[80:83], v[64:67], v[0:3]\n\t" \
  "ds_read_b64_tr_b16 v[112:113], v130 offset:8192\n\t" \
  "v_mfma_f32_16x16x32_bf16 v[16:19], v[80:83], v[68:71], v[16:19]\n\t" \
  "ds_read_b64_tr_b16 v[114:115], v130 offset:9216\n\t" \
  "v_mfma_f32_16x16x32_bf16 v[32:35], v[80:83], v[72:75], v[32:35]\n\t" \
  "ds_read_b128 v[96:99], v129 offset:0\n\t" \
  "v_mfma_f32_16x16x32_bf16 v[48:51], v[80:83], v[76:79], v[48:51]\n\t" \
  "ds_read_b128 v[100:103], v129 offset:2048\n\t" \
  "v_mfma_f32_16x16x32_bf16 v[4:7], v[84:87], v[64:67], v[4:7]\n\t" \
  "ds_read_b128 v[104:107], v129 offset:4096\n\t" \
  "v_mfma_f32_16x16x32_bf16 v[20:23], v[84:87], v[68:71], v[20:23]\n\t" \
  "ds_read_b128 v[108:111], v129 offset:6144\n\t" \
  "v_mfma_f32_16x16x32_bf16 v[36:39], v[84:87], v[72:75], v[36:39]\n\t" \
  "ds_read_b64_tr_b16 v[116:117], v131 offset:8192\n\t" \
  "v_mfma_f32_16x16x32_bf16 v[52:55], v[84:87], v[76:79], v[52:55]\n\t" \
  "ds_read_b64_tr_b16 v[118:119], v131 offset:9216\n\t" \
  "v_mfma_f32_16x16x32_bf16 v[8:11], v[88:91], v[64:67], v[8:11]\n\t" \
  "ds_read_b64_tr_b16 v[120:121], v132 offset:8192\n\t" \
  "v_mfma_f32_16x16x32_bf16 v[24:27], v[88:91], v[68:71], v[24:27]\n\t" \
  "ds_read_b64_tr_b16 v[122:123], v132 offset:9216\n\t" \
  "v_mfma_f32_16x16x32_bf16 v[40:43], v[88:91], v[72:75], v[40:43]\n\t" \
  "ds_read_b64_tr_b16 v[124:125], v133 offset:8192\n\t" \
  "v_mfma_f32_16x16x32_bf16 v[56:59], v[88:91], v[76:79], v[56:59]\n\t" \
  "ds_read_b64_tr_b16 v[126:127], v133 offset:9216\n\t" \
  "s_add_u32 %[nxt], %[cur], 49152\n\t" \
  "s_cmp_eq_u32 %[cur], 98304\n\t" \
  "s_cselect_b32 %[nxt], 0, %[nxt]\n\t" \
  "s_sub_u32 %[dl], %[nxt], %[cur]\n\t" \
  "v_mfma_f32_16x16x32_bf16 v[12:15], v[92:95], v[64:67], v[12:15]\n\t" \
  "v_add_u32 v128, %[dl], v128\n\t" \
  "v_add_u32 v129, %[dl], v129\n\t" \
  "v_mfma_f32_16x16x32_bf16 v[28:31], v[92:95], v[68:71], v[28:31]\n\t" \
  "v_add_u32 v130, %[dl], v130\n\t" \
  "v_add_u32 v131, %[dl], v131\n\t" \
  "s_add_u32 %[mb], %[lw], %[cur]\n\t" \
  "s_lshl_b32 %[tmp], %[kq], 7\n\t" \
  "s_add_u32 %[sx], %[sx0], %[tmp]\n\t" \
  "s_mul_i32 %[sw], %[kq], 245760\n\t" \
  "v_mfma_f32_16x16x32_bf16 v[44:47], v[92:95], v[72:75], v[44:47]\n\t" \
  "v_add_u32 v132, %[dl], v132\n\t" \
  "v_add_u32 v133, %[dl], v133\n\t" \
  "v_mfma_f32_16x16x32_bf16 v[60:63], v[92:95], v[76:79], v[60:63]\n\t" \
  "s_add_u32 %[kq], %[kq], 1\n\t" \
  "s_cmp_eq_u32 %[kq], 10\n\t" \
  "s_cselect_b32 %[kq], 0, %[kq]\n\t" \
  "s_mov_b32 %[cur], %[nxt]\n\t" \
  "s_waitcnt lgkmcnt(0)\n\t" \
  "s_waitcnt vmcnt(0)\n\t" \
  "LB_%=:\n\t" \
  "s_barrier\n\t" \
  "s_cmp_lt_u32 %[st], 7\n\t" \
  "s_cbranch_scc0 LND_%=\n\t" \
  "v_mfma_f32_16x16x32_bf16 v[0:3], v[112:115], v[96:99], v[0:3]\n\t" \
  "ds_read_b64_tr_b16 v[80:81], v130 offset:0\n\t" \
  "v_mfma_f32_16x16x32_bf16 v[16:19], v[112:115], v[100:103], v[16:19]\n\t" \
  "ds_read_b64_tr_b16 v[82:83], v130 offset:1024\n\t" \
  "v_mfma_f32_16x16x32_bf16 v[32:35], v[112:115], v[104:107], v[32:35]\n\t" \
  "ds_read_b128 v[64:67], v128 offset:0\n\t" \
  "s_mov_b32 m0, %[mb]\n\t" \
  "s_add_u32 %[tmp], %[sx], 0\n\t" \
  "buffer_load_dwordx4 v134, %[rx], %[tmp] offen lds\n\t" \
  "v_mfma_f32_16x16x32_bf16 v[48:51], v[112:115], v[108:111], v[48:51]\n\t" \
  "ds_read_b128 v[68:71], v128 offset:2048\n\t" \
  "v_mfma_f32_16x16x32_bf16 v[4:7], v[116:119], v[96:99], v[4:7]\n\t" \
  "ds_read_b128 v[72:75], v128 offset:4096\n\t" \
  "v_mfma_f32_16x16x32_bf16 v[20:23], v[116:119], v[100:103], v[20:23]\n\t" \
  "ds_read_b128 v[76:79], v128 offset:6144\n\t" \
  "v_mfma_f32_16x16x32_bf16 v[36:39], v[116:119], v[104:107], v[36:39]\n\t" \
  "ds_read_b64_tr_b16 v[84:85], v131 offset:0\n\t" \
  "v_mfma_f32_16x16x32_bf16 v[52:55], v[116:119], v[108:111], v[52:55]\n\t" \
  "ds_read_b64_tr_b16 v[86:87], v131 offset:1024\n\t" \
  "s_add_u32 m0, %[mb], 8192\n\t" \
  "s_add_u32 %[tmp], %[sx], 81920\n\t" \
  "buffer_load_dwordx4 v134, %[rx], %[tmp] offen lds\n\t" \
  "v_mfma_f32_16x16x32_bf16 v[8:11], v[120:123], v[96:99], v[8:11]\n\t" \
  "ds_read_b64_tr_b16 v[88:89], v132 offset:0\n\t" \
  "v_mfma_f32_16x16x32_bf16 v[24:27], v[120:123], v[100:103], v[24:27]\n\t" \
  "ds_read_b64_tr_b16 v[90:91], v132 offset:1024\n\t" \
  "v_mfma_f32_16x16x32_bf16 v[40:43], v[120:123], v[104:107], v[40:43]\n\t" \
  "ds_read_b64_tr_b16 v[92:93], v133 offset:0\n\t" \
  "v_mfma_f32_16x16x32_bf16 v[56:59], v[120:123], v[108:111], v[56:59]\n\t" \
  "ds_read_b64_tr_b16 v[94:95], v133 offset:1024\n\t" \
  "v_mfma_f32_16x16x32_bf16 v[12:15], v[124:127], v[96:99], v[12:15]\n\t" \
  "s_add_u32 m0, %[mb], 16384\n\t" \
  "s_add_u32 %[tmp], %[sx], 163840\n\t" \
  "buffer_load_dwordx4 v134, %[rx], %[tmp] offen lds\n\t" \
  "v_mfma_f32_16x16x32_bf16 v[28:31], v[124:127], v[100:103], v[28:31]\n\t" \
  "v_mfma_f32_16x16x32_bf16 v[44:47], v[124:127], v[104:107], v[44:47]\n\t" \
  "v_mfma_f32_16x16x32_bf16 v[60:63], v[124:127], v[108:111], v[60:63]\n\t" \
  "s_branch LJ_%=\n\t" \
  "LND_%=:\n\t" \
  "v_mfma_f32_16x16x32_bf16 v[0:3], v[112:115], v[96:99], v[0:3]\n\t" \
  "ds_read_b64_tr_b16 v[80:81], v130 offset:0\n\t" \
  "v_mfma_f32_16x16x32_bf16 v[16:19], v[112:115], v[100:103], v[16:19]\n\t" \
  "ds_read_b64_tr_b16 v[82:83], v130 offset:1024\n\t" \
  "v_mfma_f32_16x16x32_bf16 v[32:35], v[112:115], v[104:107], v[32:35]\n\t" \
  "ds_read_b128 v[64:67], v128 offset:0\n\t" \
  "v_mfma_f32_16x16x32_bf16 v[48:51], v[112:115], v[108:111], v[48:51]\n\t" \
  "ds_read_b128 v[68:71], v128 offset:2048\n\t" \
  "v_mfma_f32_16x16x32_bf16 v[4:7], v[116:119], v[96:99], v[4:7]\n\t" \
  "ds_read_b128 v[72:75], v128 offset:4096\n\t" \
  "v_mfma_f32_16x16x32_bf16 v[20:23], v[116:119], v[100:103], v[20:23]\n\t" \
  "ds_read_b128 v[76:79], v128 offset:6144\n\t" \
  "v_mfma_f32_16x16x32_bf16 v[36:39], v[116:119], v[104:107], v[36:39]\n\t" \
  "ds_read_b64_tr_b16 v[84:85], v131 offset:0\n\t" \
  "v_mfma_f32_16x16x32_bf16 v[52:55], v[116:119], v[108:111], v[52:55]\n\t" \
  "ds_read_b64_tr_b16 v[86:87], v131 offset:1024\n\t" \
  "v_mfma_f32_16x16x32_bf16 v[8:11], v[120:123], v[96:99], v[8:11]\n\t" \
  "ds_read_b64_tr_b16 v[88:89], v132 offset:0\n\t" \
  "v_mfma_f32_16x16x32_bf16 v[24:27], v[120:123], v[100:103], v[24:27]\n\t" \
  "ds_read_b64_tr_b16 v[90:91], v132 offset:1024\n\t" \
  "v_mfma_f32_16x16x32_bf16 v[40:43], v[120:123], v[104:107], v[40:43]\n\t" \
  "ds_read_b64_tr_b16 v[92:93], v133 offset:0\n\t" \
  "v_mfma_f32_16x16x32_bf16 v[56:59], v[120:123], v[108:111], v[56:59]\n\t" \
  "ds_read_b64_tr_b16 v[94:95], v133 offset:1024\n\t" \
  "v_mfma_f32_16x16x32_bf16 v[12:15], v[124:127], v[96:99], v[12:15]\n\t" \
  "v_mfma_f32_16x16x32_bf16 v[28:31], v[124:127], v[100:103], v[28:31]\n\t" \
  "v_mfma_f32_16x16x32_bf16 v[44:47], v[124:127], v[104:107], v[44:47]\n\t" \
  "v_mfma_f32_16x16x32_bf16 v[60:63], v[124:127], v[108:111], v[60:63]\n\t" \
  "LJ_%=:\n\t" \
  "s_add_u32 %[st], %[st], 1\n\t" \
  "s_cmp_lt_u32 %[st], 10\n\t" \
  "s_cbranch_scc1 LP1_%=\n\t" \
  "s_waitcnt lgkmcnt(0)\n\t" \
  "buffer_load_dwordx2 v[192:193], off, %[rfl], %[fe] sc1\n\t" \
  "v_cvt_pk_bf16_f32 v64, v4, v5\n\t" \
  "v_cvt_pk_bf16_f32 v65, v6, v7\n\t" \
  "v_cvt_pk_bf16_f32 v66, v20, v21\n\t" \
  "v_cvt_pk_bf16_f32 v67, v22, v23\n\t" \
  "v_pk_mul_f32 v[8:9], v[8:9], v[212:213] op_sel_hi:[1,0]\n\t" \
  "v_pk_mul_f32 v[10:11], v[10:11], v[212:213] op_sel_hi:[1,0]\n\t" \
  "v_pk_mul_f32 v[24:25], v[24:25], v[212:213] op_sel_hi:[1,0]\n\t" \
  "v_pk_mul_f32 v[26:27], v[26:27], v[212:213] op_sel_hi:[1,0]\n\t" \
  "v_permlane16_swap_b32 v64, v66\n\t" \
  "v_permlane16_swap_b32 v65, v67\n\t" \
  "v_cvt_pk_bf16_f32 v68, v8, v9\n\t" \
  "v_cvt_pk_bf16_f32 v69, v10, v11\n\t" \
  "v_cvt_pk_bf16_f32 v70, v12, v13\n\t" \
  "v_cvt_pk_bf16_f32 v71, v14, v15\n\t" \
  "buffer_store_dwordx4 v[64:67], v197, %[rkv], %[gim] offen sc1\n\t" \
  "ds_write_b128 v199, v[64:67]\n\t" \
  "v_cvt_pk_bf16_f32 v72, v24, v25\n\t" \
  "v_cvt_pk_bf16_f32 v73, v26, v27\n\t" \
  "v_cvt_pk_bf16_f32 v74, v28, v29\n\t" \
  "v_cvt_pk_bf16_f32 v75, v30, v31\n\t" \
  "v_mfma_f32_16x16x32_bf16 v[76:79], v[68:71], v[200:203], 0\n\t" \
  "v_mfma_f32_16x16x32_bf16 v[84:87], v[68:71], v[204:207], v[208:211]\n\t" \
  "v_cvt_pk_bf16_f32 v100, v36, v37\n\t" \
  "v_cvt_pk_bf16_f32 v101, v38, v39\n\t" \
  "v_cvt_pk_bf16_f32 v102, v52, v53\n\t" \
  "v_cvt_pk_bf16_f32 v103, v54, v55\n\t" \
  "v_mfma_f32_16x16x32_bf16 v[80:83], v[72:75], v[200:203], 0\n\t" \
  "v_mfma_f32_16x16x32_bf16 v[88:91], v[72:75], v[204:207], v[208:211]\n\t" \
  "v_pk_mul_f32 v[40:41], v[40:41], v[212:213] op_sel_hi:[1,0]\n\t" \
  "v_pk_mul_f32 v[42:43], v[42:43], v[212:213] op_sel_hi:[1,0]\n\t" \
  "v_pk_mul_f32 v[56:57], v[56:57], v[212:213] op_sel_hi:[1,0]\n\t" \
  "v_pk_mul_f32 v[58:59], v[58:59], v[212:213] op_sel_hi:[1,0]\n\t" \
  "v_permlane16_swap_b32 v100, v102\n\t" \
  "v_permlane16_swap_b32 v101, v103\n\t" \
  "v_cvt_pk_bf16_f32 v104, v40, v41\n\t" \
  "v_cvt_pk_bf16_f32 v105, v42, v43\n\t" \
  "v_cvt_pk_bf16_f32 v106, v44, v45\n\t" \
  "v_cvt_pk_bf16_f32 v107, v46, v47\n\t" \
  "buffer_store_dwordx4 v[100:103], v197, %[rkv], %[gim] offen offset:1024 sc1\n\t" \
  "ds_write_b128 v199, v[100:103] offset:1024\n\t" \
  "v_cvt_pk_bf16_f32 v108, v56, v57\n\t" \
  "v_cvt_pk_bf16_f32 v109, v58, v59\n\t" \
  "v_cvt_pk_bf16_f32 v110, v60, v61\n\t" \
  "v_cvt_pk_bf16_f32 v111, v62, v63\n\t" \
  "v_cvt_pk_bf16_f32 v92, v76, v77\n\t" \
  "v_cvt_pk_bf16_f32 v93, v78, v79\n\t" \
  "v_cvt_pk_bf16_f32 v94, v80, v81\n\t" \
  "v_cvt_pk_bf16_f32 v95, v82, v83\n\t" \
  "v_mfma_f32_16x16x32_bf16 v[112:115], v[104:107], v[200:203], 0\n\t" \
  "v_mfma_f32_16x16x32_bf16 v[120:123], v[104:107], v[204:207], v[208:211]\n\t" \
  "buffer_store_dwordx4 v[92:95], v198, %[rkv], %[gim] offen sc1\n\t" \
  "ds_write_b128 v213, v[92:95]\n\t" \
  "v_mfma_f32_16x16x32_bf16 v[116:119], v[108:111], v[200:203], 0\n\t" \
  "v_mfma_f32_16x16x32_bf16 v[124:127], v[108:111], v[204:207], v[208:211]\n\t" \
  "v_cvt_pk_bf16_f32 v96, v84, v85\n\t" \
  "v_cvt_pk_bf16_f32 v97, v86, v87\n\t" \
  "v_cvt_pk_bf16_f32 v98, v88, v89\n\t" \
  "v_cvt_pk_bf16_f32 v99, v90, v91\n\t" \
  "s_mov_b64 exec, %[m6]\n\t" \
  "buffer_store_dwordx4 v[96:99], v198, %[rkv], %[gim] offen offset:256 sc1\n\t" \
  "ds_write_b128 v213, v[96:99] offset:256\n\t" \
  "s_mov_b64 exec, -1\n\t" \
  "v_permlane16_swap_b32 v68, v72\n\t" \
  "v_permlane16_swap_b32 v69, v73\n\t" \
  "v_permlane16_swap_b32 v104, v108\n\t" \
  "v_permlane16_swap_b32 v105, v109\n\t" \
  "v_cvt_pk_bf16_f32 v4, v112, v113\n\t" \
  "v_cvt_pk_bf16_f32 v5, v114, v115\n\t" \
  "v_cvt_pk_bf16_f32 v6, v116, v117\n\t" \
  "v_cvt_pk_bf16_f32 v7, v118, v119\n\t" \
  "buffer_store_dwordx4 v[4:7], v198, %[rkv], %[gim] offen offset:1408 sc1\n\t" \
  "ds_write_b128 v213, v[4:7] offset:1408\n\t" \
  "v_cvt_pk_bf16_f32 v20, v120, v121\n\t" \
  "v_cvt_pk_bf16_f32 v21, v122, v123\n\t" \
  "v_cvt_pk_bf16_f32 v22, v124, v125\n\t" \
  "v_cvt_pk_bf16_f32 v23, v126, v127\n\t" \
  "s_mov_b64 exec, %[m6]\n\t" \
  "buffer_store_dwordx4 v[20:23], v198, %[rkv], %[gim] offen offset:1664 sc1\n\t" \
  "ds_write_b128 v213, v[20:23] offset:1664\n\t" \
  "s_mov_b64 exec, -1\n\t" \
  "s_mov_b32 exec_hi, 0\n\t" \
  "v_mov_b64 v[36:37], v[72:73]\n\t" \
  "v_mov_b64 v[38:39], v[108:109]\n\t" \
  "v_mov_b64 v[220:221], v[68:69]\n\t" \
  "v_mov_b64 v[226:227], v[104:105]\n\t" \
  "buffer_store_dwordx4 v[36:39], v197, %[rkv], %[gim] offen offset:2048 sc1\n\t" \
  "ds_write_b128 v199, v[36:39] offset:2048\n\t" \
  "s_mov_b64 exec, -1\n\t" \
  "v_pk_mul_f32 v[0:1], v[0:1], %[qs] op_sel_hi:[1,0]\n\t" \
  "v_pk_mul_f32 v[2:3], v[2:3], %[qs] op_sel_hi:[1,0]\n\t" \
  "v_pk_mul_f32 v[16:17], v[16:17], %[qs] op_sel_hi:[1,0]\n\t" \
  "v_pk_mul_f32 v[18:19], v[18:19], %[qs] op_sel_hi:[1,0]\n\t" \
  "v_cvt_pk_bf16_f32 v216, v0, v1\n\t" \
  "v_cvt_pk_bf16_f32 v217, v2, v3\n\t" \
  "v_cvt_pk_bf16_f32 v218, v16, v17\n\t" \
  "v_cvt_pk_bf16_f32 v219, v18, v19\n\t" \
  "s_nop 0\n\t" \
  "v_permlane16_swap_b32 v216, v218\n\t" \
  "v_permlane16_swap_b32 v217, v219\n\t" \
  "s_waitcnt lgkmcnt(0)\n\t" \
  "s_barrier\n\t" \
  "ds_read_b128 v[80:83], v194\n\t" \
  "ds_read_b128 v[84:87], v195\n\t" \
  "v_pk_mul_f32 v[32:33], v[32:33], %[qs] op_sel_hi:[1,0]\n\t" \
  "v_pk_mul_f32 v[34:35], v[34:35], %[qs] op_sel_hi:[1,0]\n\t" \
  "v_pk_mul_f32 v[48:49], v[48:49], %[qs] op_sel_hi:[1,0]\n\t" \
  "v_pk_mul_f32 v[50:51], v[50:51], %[qs] op_sel_hi:[1,0]\n\t" \
  "v_cvt_pk_bf16_f32 v222, v32, v33\n\t" \
  "v_cvt_pk_bf16_f32 v223, v34, v35\n\t" \
  "v_cvt_pk_bf16_f32 v224, v48, v49\n\t" \
  "v_cvt_pk_bf16_f32 v225, v50, v51\n\t" \
  "s_waitcnt lgkmcnt(0)\n\t" \
  "v_mfma_f32_32x32x16_bf16 v[32:47], v[80:83], v[216:219], 0\n\t" \
  "v_permlane16_swap_b32 v222, v224\n\t" \
  "v_permlane16_swap_b32 v223, v225\n\t" \
  "v_mfma_f32_32x32x8_bf16 v[0:15], v[84:85], v[220:221], v[32:47]\n\t" \
  "s_nop 0\n\t" \
  "v_mfma_f32_32x32x16_bf16 v[48:63], v[80:83], v[222:225], 0\n\t" \
  "v_mfma_f32_32x32x8_bf16 v[16:31], v[84:85], v[226:227], v[48:63]\n\t" \
  "s_nop 8\n\t" \
  "v_max3_f32 v68, v0, v1, v2\n\t" \
  "v_max3_f32 v68, v68, v3, v4\n\t" \
  "v_max3_f32 v68, v68, v5, v6\n\t" \
  "v_max3_f32 v68, v68, v7, v8\n\t" \
  "v_max3_f32 v68, v68, v9, v10\n\t" \
  "v_max3_f32 v69, v16, v17, v18\n\t" \
  "v_max3_f32 v68, v68, v11, v12\n\t" \
  "v_max3_f32 v69, v69, v19, v20\n\t" \
  "v_max3_f32 v68, v68, v13, v14\n\t" \
  "v_max3_f32 v69, v69, v21, v22\n\t" \
  "v_max_f32 v68, v68, v15\n\t" \
  "v_max3_f32 v69, v69, v23, v24\n\t" \
  "v_max3_f32 v69, v69, v25, v26\n\t" \
  "v_max3_f32 v69, v69, v27, v28\n\t" \
  "v_max3_f32 v69, v69, v29, v30\n\t" \
  "v_max_f32 v69, v69, v31\n\t" \
  "v_mov_b32 v70, v68\n\t" \
  "v_mov_b32 v71, v69\n\t" \
  "s_nop 0\n\t" \
  "v_permlane32_swap_b32 v68, v70\n\t" \
  "v_permlane32_swap_b32 v69, v71\n\t" \
  "v_max_f32 v68, v68, v70\n\t" \
  "v_max_f32 v69, v69, v71\n\t" \
  "s_mov_b32 exec_lo, 0\n\t" \
  "v_cvt_pk_bf16_f32 v220, -v68, 0\n\t" \
  "v_cvt_pk_bf16_f32 v226, -v69, 0\n\t" \
  "s_mov_b64 exec, -1\n\t" \
  "s_nop 0\n\t" \
  "v_mfma_f32_32x32x8_bf16 v[0:15], v[84:85], v[220:221], v[32:47]\n\t" \
  "s_mov_b32 %[st], 0\n\t" \
  "s_mov_b32 %[cur], 86016\n\t" \
  "s_cmp_lt_u32 %[w1k], 4096\n\t" \
  "s_cbranch_scc0 LHI_%=\n\t" \
  "s_mul_i32 %[wsh], %[w1k], 21\n\t" \
  "s_lshr_b32 %[wsh], %[wsh], 1\n\t" \
  "s_sub_u32 %[lsh], %[lw], %[w1k]\n\t" \
  "s_add_u32 %[lsh], %[lsh], %[wsh]\n\t" \
  "LAa_%=:\n\t" \
  "s_add_u32 %[nxt], %[cur], 43008\n\t" \
  "s_cmp_eq_u32 %[cur], 86016\n\t" \
  "s_cselect_b32 %[nxt], 0, %[nxt]\n\t" \
  "s_sub_u32 %[dl], %[nxt], %[cur]\n\t" \
  "s_add_u32 %[mb], %[lsh], %[nxt]\n\t" \
  "s_add_u32 %[tmp], %[e1], %[st]\n\t" \
  "s_and_b32 %[tmp], %[tmp], 7\n\t" \
  "s_mul_i32 %[sx], %[tmp], 43008\n\t" \
  "s_add_u32 %[sx], %[sx], %[wsh]\n\t" \
  "s_mov_b32 %[cur], %[nxt]\n\t" \
  "s_bitcmp1_b32 0x81, %[st]\n\t" \
  "s_cselect_b64 vcc, -1, 0\n\t" \
  "v_mfma_f32_32x32x16_bf16 v[16:31], v[80:83], v[222:225], 0\n\t" \
  "v_exp_f32 v0, v0\n\t" \
  "v_exp_f32 v1, v1\n\t" \
  "v_exp_f32 v2, v2\n\t" \
  "v_exp_f32 v3, v3\n\t" \
  "v_mfma_f32_32x32x8_bf16 v[16:31], v[84:85], v[226:227], v[16:31]\n\t" \
  "ds_read_b128 v[80:83], v194 offset:1024\n\t" \
  "v_exp_f32 v4, v4\n\t" \
  "v_exp_f32 v5, v5\n\t" \
  "v_exp_f32 v6, v6\n\t" \
  "v_exp_f32 v7, v7\n\t" \
  "v_smfmac_f32_32x32x32_bf16 v[152:167], v[176:179], v[48:55], v168\n\t" \
  "ds_read_b128 v[176:179], v196 offset:0\n\t" \
  "v_exp_f32 v8, v8\n\t" \
  "v_exp_f32 v9, v9\n\t" \
  "v_exp_f32 v10, v10\n\t" \
  "v_exp_f32 v11, v11\n\t" \
  "v_smfmac_f32_32x32x32_bf16 v[152:167], v[180:183], v[56:63], v168\n\t" \
  "ds_read_b128 v[180:183], v196 offset:704\n\t" \
  "v_exp_f32 v12, v12\n\t" \
  "v_exp_f32 v13, v13\n\t" \
  "v_exp_f32 v14, v14\n\t" \
  "v_exp_f32 v15, v15\n\t" \
  "s_waitcnt lgkmcnt(2)\n\t" \
  "v_mfma_f32_32x32x16_bf16 v[32:47], v[80:83], v[216:219], 0\n\t" \
  "v_exp_f32 v16, v16\n\t" \
  "v_exp_f32 v17, v17\n\t" \
  "v_exp_f32 v18, v18\n\t" \
  "v_exp_f32 v19, v19\n\t" \
  "v_mfma_f32_32x32x8_bf16 v[32:47], v[86:87], v[220:221], v[32:47]\n\t" \
  "v_exp_f32 v20, v20\n\t" \
  "v_exp_f32 v21, v21\n\t" \
  "v_exp_f32 v22, v22\n\t" \
  "v_exp_f32 v23, v23\n\t" \
  "s_bitcmp1_b32 0x87, %[st]\n\t" \
  "s_cbranch_scc1 LHa0_%=\n\t" \
  "LGa0_%=:\n\t" \
  "s_mov_b32 m0, %[mb]\n\t" \
  "s_mov_b32 %[tmp], %[sx]\n\t" \
  "buffer_load_dwordx4 v189, %[rkv], %[tmp] offen sc1 lds\n\t" \
  "buffer_load_dwordx4 v189, %[rkv], %[tmp] offen offset:1024 sc1 lds\n\t" \
  "buffer_load_dwordx4 v189, %[rkv], %[tmp] offen offset:2048 sc1 lds\n\t" \
  "buffer_load_dwordx4 v189, %[rkv], %[tmp] offen offset:3072 sc1 lds\n\t" \
  "LRa0_%=:\n\t" \
  "s_waitcnt lgkmcnt(0)\n\t" \
  "v_smfmac_f32_32x32x32_bf16 v[136:151], v[176:179], v[0:7], v168\n\t" \
  "v_exp_f32 v24, v24\n\t" \
  "v_exp_f32 v25, v25\n\t" \
  "v_exp_f32 v26, v26\n\t" \
  "v_exp_f32 v27, v27\n\t" \
  "v_smfmac_f32_32x32x32_bf16 v[136:151], v[180:183], v[8:15], v168\n\t" \
  "v_exp_f32 v28, v28\n\t" \
  "v_exp_f32 v29, v29\n\t" \
  "v_exp_f32 v30, v30\n\t" \
  "v_exp_f32 v31, v31\n\t" \
  "v_mfma_f32_32x32x16_bf16 v[48:63], v[80:83], v[222:225], 0\n\t" \
  "v_exp_f32 v32, v32\n\t" \
  "v_exp_f32 v33, v33\n\t" \
  "v_exp_f32 v34, v34\n\t" \
  "v_exp_f32 v35, v35\n\t" \
  "v_mfma_f32_32x32x8_bf16 v[48:63], v[86:87], v[226:227], v[48:63]\n\t" \
  "ds_read_b128 v[80:83], v194 offset:5376\n\t" \
  "ds_read_b128 v[84:87], v195 offset:5376\n\t" \
  "v_exp_f32 v36, v36\n\t" \
  "v_exp_f32 v37, v37\n\t" \
  "v_exp_f32 v38, v38\n\t" \
  "v_exp_f32 v39, v39\n\t" \
  "v_smfmac_f32_32x32x32_bf16 v[152:167], v[176:179], v[16:23], v168\n\t" \
  "ds_read_b128 v[176:179], v196 offset:1408\n\t" \
  "v_exp_f32 v40, v40\n\t" \
  "v_exp_f32 v41, v41\n\t" \
  "v_exp_f32 v42, v42\n\t" \
  "v_exp_f32 v43, v43\n\t" \
  "v_smfmac_f32_32x32x32_bf16 v[152:167], v[180:183], v[24:31], v168\n\t" \
  "ds_read_b128 v[180:183], v196 offset:2112\n\t" \
  "v_exp_f32 v44, v44\n\t" \
  "v_exp_f32 v45, v45\n\t" \
  "v_exp_f32 v46, v46\n\t" \
  "v_exp_f32 v47, v47\n\t" \
  "s_waitcnt lgkmcnt(2)\n\t" \
  "v_mfma_f32_32x32x16_bf16 v[0:15], v[80:83], v[216:219], 0\n\t" \
  "v_exp_f32 v48, v48\n\t" \
  "v_exp_f32 v49, v49\n\t" \
  "v_exp_f32 v50, v50\n\t" \
  "v_exp_f32 v51, v51\n\t" \
  "v_mfma_f32_32x32x8_bf16 v[0:15], v[84:85], v[220:221], v[0:15]\n\t" \
  "v_exp_f32 v52, v52\n\t" \
  "v_exp_f32 v53, v53\n\t" \
  "v_exp_f32 v54, v54\n\t" \
  "v_exp_f32 v55, v55\n\t" \
  "s_cbranch_vccnz LHa1_%=\n\t" \
  "LGa1_%=:\n\t" \
  "s_add_u32 m0, %[mb], 4096\n\t" \
  "s_add_u32 %[tmp], %[sx], 4096\n\t" \
  "buffer_load_dwordx4 v189, %[rkv], %[tmp] offen sc1 lds\n\t" \
  "buffer_load_dwordx4 v189, %[rkv], %[tmp] offen offset:1024 sc1 lds\n\t" \
  "buffer_load_dwordx4 v189, %[rkv], %[tmp] offen offset:2048 sc1 lds\n\t" \
  "buffer_load_dwordx4 v189, %[rkv], %[tmp] offen offset:3072 sc1 lds\n\t" \
  "LRa1_%=:\n\t" \
  "s_waitcnt lgkmcnt(0)\n\t" \
  "v_smfmac_f32_32x32x32_bf16 v[136:151], v[176:179], v[32:39], v168\n\t" \
  "v_exp_f32 v56, v56\n\t" \
  "v_exp_f32 v57, v57\n\t" \
  "v_exp_f32 v58, v58\n\t" \
  "v_exp_f32 v59, v59\n\t" \
  "v_smfmac_f32_32x32x32_bf16 v[136:151], v[180:183], v[40:47], v168\n\t" \
  "v_exp_f32 v60, v60\n\t" \
  "v_exp_f32 v61, v61\n\t" \
  "v_exp_f32 v62, v62\n\t" \
  "v_exp_f32 v63, v63\n\t" \
  "v_mfma_f32_32x32x16_bf16 v[16:31], v[80:83], v[222:225], 0\n\t" \
  "v_exp_f32 v0, v0\n\t" \
  "v_exp_f32 v1, v1\n\t" \
  "v_exp_f32 v2, v2\n\t" \
  "v_exp_f32 v3, v3\n\t" \
  "v_mfma_f32_32x32x8_bf16 v[16:31], v[84:85], v[226:227], v[16:31]\n\t" \
  "ds_read_b128 v[80:83], v194 offset:6400\n\t" \
  "v_exp_f32 v4, v4\n\t" \
  "v_exp_f32 v5, v5\n\t" \
  "v_exp_f32 v6, v6\n\t" \
  "v_exp_f32 v7, v7\n\t" \
  "v_smfmac_f32_32x32x32_bf16 v[152:167], v[176:179], v[48:55], v168\n\t" \
  "ds_read_b128 v[176:179], v196 offset:5376\n\t" \
  "v_exp_f32 v8, v8\n\t" \
  "v_exp_f32 v9, v9\n\t" \
  "v_exp_f32 v10, v10\n\t" \
  "v_exp_f32 v11, v11\n\t" \
  "v_smfmac_f32_32x32x32_bf16 v[152:167], v[180:183], v[56:63], v168\n\t" \
  "ds_read_b128 v[180:183], v196 offset:6080\n\t" \
  "v_exp_f32 v12, v12\n\t" \
  "v_exp_f32 v13, v13\n\t" \
  "v_exp_f32 v14, v14\n\t" \
  "v_exp_f32 v15, v15\n\t" \
  "s_waitcnt lgkmcnt(2)\n\t" \
  "v_mfma_f32_32x32x16_bf16 v[32:47], v[80:83], v[216:219], 0\n\t" \
  "v_exp_f32 v16, v16\n\t" \
  "v_exp_f32 v17, v17\n\t" \
  "v_exp_f32 v18, v18\n\t" \
  "v_exp_f32 v19, v19\n\t" \
  "v_mfma_f32_32x32x8_bf16 v[32:47], v[86:87], v[220:221], v[32:47]\n\t" \
  "v_exp_f32 v20, v20\n\t" \
  "v_exp_f32 v21, v21\n\t" \
  "v_exp_f32 v22, v22\n\t" \
  "v_exp_f32 v23, v23\n\t" \
  "s_cbranch_vccnz LHa2_%=\n\t" \
  "LGa2_%=:\n\t" \
  "s_add_u32 m0, %[mb], 8192\n\t" \
  "s_add_u32 %[tmp], %[sx], 8192\n\t" \
  "buffer_load_dwordx4 v189, %[rkv], %[tmp] offen sc1 lds\n\t" \
  "buffer_load_dwordx4 v189, %[rkv], %[tmp] offen offset:1024 sc1 lds\n\t" \
  "buffer_load_dwordx4 v189, %[rkv], %[tmp] offen offset:1536 sc1 lds\n\t" \
  "LRa2_%=:\n\t" \
  "s_waitcnt lgkmcnt(0)\n\t" \
  "v_smfmac_f32_32x32x32_bf16 v[136:151], v[176:179], v[0:7], v168\n\t" \
  "v_exp_f32 v24, v24\n\t" \
  "v_exp_f32 v25, v25\n\t" \
  "v_exp_f32 v26, v26\n\t" \
  "v_exp_f32 v27, v27\n\t" \
  "v_smfmac_f32_32x32x32_bf16 v[136:151], v[180:183], v[8:15], v168\n\t" \
  "v_exp_f32 v28, v28\n\t" \
  "v_exp_f32 v29, v29\n\t" \
  "v_exp_f32 v30, v30\n\t" \
  "v_exp_f32 v31, v31\n\t" \
  "v_mfma_f32_32x32x16_bf16 v[48:63], v[80:83], v[222:225], 0\n\t" \
  "v_exp_f32 v32, v32\n\t" \
  "v_exp_f32 v33, v33\n\t" \
  "v_exp_f32 v34, v34\n\t" \
  "v_exp_f32 v35, v35\n\t" \
  "v_mfma_f32_32x32x8_bf16 v[48:63], v[86:87], v[226:227], v[48:63]\n\t" \
  "ds_read_b128 v[80:83], v194 offset:10752\n\t" \
  "ds_read_b128 v[84:87], v195 offset:10752\n\t" \
  "v_exp_f32 v36, v36\n\t" \
  "v_exp_f32 v37, v37\n\t" \
  "v_exp_f32 v38, v38\n\t" \
  "v_exp_f32 v39, v39\n\t" \
  "v_smfmac_f32_32x32x32_bf16 v[152:167], v[176:179], v[16:23], v168\n\t" \
  "ds_read_b128 v[176:179], v196 offset:6784\n\t" \
  "v_exp_f32 v40, v40\n\t" \
  "v_exp_f32 v41, v41\n\t" \
  "v_exp_f32 v42, v42\n\t" \
  "v_exp_f32 v43, v43\n\t" \
  "v_smfmac_f32_32x32x32_bf16 v[152:167], v[180:183], v[24:31], v168\n\t" \
  "ds_read_b128 v[180:183], v196 offset:7488\n\t" \
  "v_exp_f32 v44, v44\n\t" \
  "v_exp_f32 v45, v45\n\t" \
  "v_exp_f32 v46, v46\n\t" \
  "v_exp_f32 v47, v47\n\t" \
  "s_waitcnt lgkmcnt(2)\n\t" \
  "v_mfma_f32_32x32x16_bf16 v[0:15], v[80:83], v[216:219], 0\n\t" \
  "v_exp_f32 v48, v48\n\t" \
  "v_exp_f32 v49, v49\n\t" \
  "v_exp_f32 v50, v50\n\t" \
  "v_exp_f32 v51, v51\n\t" \
  "v_mfma_f32_32x32x8_bf16 v[0:15], v[84:85], v[220:221], v[0:15]\n\t" \
  "v_exp_f32 v52, v52\n\t" \
  "v_exp_f32 v53, v53\n\t" \
  "v_exp_f32 v54, v54\n\t" \
  "v_exp_f32 v55, v55\n\t" \
  "s_cbranch_vccnz LHa3_%=\n\t" \
  "LGa3_%=:\n\t" \
  "LRa3_%=:\n\t" \
  "s_waitcnt lgkmcnt(0)\n\t" \
  "v_smfmac_f32_32x32x32_bf16 v[136:151], v[176:179], v[32:39], v168\n\t" \
  "v_exp_f32 v56, v56\n\t" \
  "v_exp_f32 v57, v57\n\t" \
  "v_exp_f32 v58, v58\n\t" \
  "v_exp_f32 v59, v59\n\t" \
  "v_smfmac_f32_32x32x32_bf16 v[136:151], v[180:183], v[40:47], v168\n\t" \
  "v_exp_f32 v60, v60\n\t" \
  "v_exp_f32 v61, v61\n\t" \
  "v_exp_f32 v62, v62\n\t" \
  "v_exp_f32 v63, v63\n\t" \
  "v_mfma_f32_32x32x16_bf16 v[16:31], v[80:83], v[222:225], 0\n\t" \
  "v_exp_f32 v0, v0\n\t" \
  "v_exp_f32 v1, v1\n\t" \
  "v_exp_f32 v2, v2\n\t" \
  "v_exp_f32 v3, v3\n\t" \
  "v_mfma_f32_32x32x8_bf16 v[16:31], v[84:85], v[226:227], v[16:31]\n\t" \
  "ds_read_b128 v[80:83], v194 offset:11776\n\t" \
  "v_exp_f32 v4, v4\n\t" \
  "v_exp_f32 v5, v5\n\t" \
  "v_exp_f32 v6, v6\n\t" \
  "v_exp_f32 v7, v7\n\t" \
  "v_smfmac_f32_32x32x32_bf16 v[152:167], v[176:179], v[48:55], v168\n\t" \
  "ds_read_b128 v[176:179], v196 offset:10752\n\t" \
  "v_exp_f32 v8, v8\n\t" \
  "v_exp_f32 v9, v9\n\t" \
  "v_exp_f32 v10, v10\n\t" \
  "v_exp_f32 v11, v11\n\t" \
  "v_smfmac_f32_32x32x32_bf16 v[152:167], v[180:183], v[56:63], v168\n\t" \
  "ds_read_b128 v[180:183], v196 offset:11456\n\t" \
  "v_exp_f32 v12, v12\n\t" \
  "v_exp_f32 v13, v13\n\t" \
  "v_exp_f32 v14, v14\n\t" \
  "v_exp_f32 v15, v15\n\t" \
  "s_waitcnt lgkmcnt(2)\n\t" \
  "v_mfma_f32_32x32x16_bf16 v[32:47], v[80:83], v[216:219], 0\n\t" \
  "v_exp_f32 v16, v16\n\t" \
  "v_exp_f32 v17, v17\n\t" \
  "v_exp_f32 v18, v18\n\t" \
  "v_exp_f32 v19, v19\n\t" \
  "v_mfma_f32_32x32x8_bf16 v[32:47], v[86:87], v[220:221], v[32:47]\n\t" \
  "v_exp_f32 v20, v20\n\t" \
  "v_exp_f32 v21, v21\n\t" \
  "v_exp_f32 v22, v22\n\t" \
  "v_exp_f32 v23, v23\n\t" \
  "s_waitcnt lgkmcnt(0)\n\t" \
  "v_smfmac_f32_32x32x32_bf16 v[136:151], v[176:179], v[0:7], v168\n\t" \
  "v_exp_f32 v24, v24\n\t" \
  "v_exp_f32 v25, v25\n\t" \
  "v_exp_f32 v26, v26\n\t" \
  "v_exp_f32 v27, v27\n\t" \
  "v_smfmac_f32_32x32x32_bf16 v[136:151], v[180:183], v[8:15], v168\n\t" \
  "v_exp_f32 v28, v28\n\t" \
  "v_exp_f32 v29, v29\n\t" \
  "v_exp_f32 v30, v30\n\t" \
  "v_exp_f32 v31, v31\n\t" \
  "v_mfma_f32_32x32x16_bf16 v[48:63], v[80:83], v[222:225], 0\n\t" \
  "v_exp_f32 v32, v32\n\t" \
  "v_exp_f32 v33, v33\n\t" \
  "v_exp_f32 v34, v34\n\t" \
  "v_exp_f32 v35, v35\n\t" \
  "v_mfma_f32_32x32x8_bf16 v[48:63], v[86:87], v[226:227], v[48:63]\n\t" \
  "ds_read_b128 v[80:83], v194 offset:16128\n\t" \
  "ds_read_b128 v[84:87], v195 offset:16128\n\t" \
  "v_exp_f32 v36, v36\n\t" \
  "v_exp_f32 v37, v37\n\t" \
  "v_exp_f32 v38, v38\n\t" \
  "v_exp_f32 v39, v39\n\t" \
  "v_smfmac_f32_32x32x32_bf16 v[152:167], v[176:179], v[16:23], v168\n\t" \
  "ds_read_b128 v[176:179], v196 offset:12160\n\t" \
  "v_exp_f32 v40, v40\n\t" \
  "v_exp_f32 v41, v41\n\t" \
  "v_exp_f32 v42, v42\n\t" \
  "v_exp_f32 v43, v43\n\t" \
  "v_smfmac_f32_32x32x32_bf16 v[152:167], v[180:183], v[24:31], v168\n\t" \
  "ds_read_b128 v[180:183], v196 offset:12864\n\t" \
  "v_exp_f32 v44, v44\n\t" \
  "v_exp_f32 v45, v45\n\t" \
  "v_exp_f32 v46, v46\n\t" \
  "v_exp_f32 v47, v47\n\t" \
  "s_waitcnt lgkmcnt(2)\n\t" \
  "v_mfma_f32_32x32x16_bf16 v[0:15], v[80:83], v[216:219], 0\n\t" \
  "v_exp_f32 v48, v48\n\t" \
  "v_exp_f32 v49, v49\n\t" \
  "v_exp_f32 v50, v50\n\t" \
  "v_exp_f32 v51, v51\n\t" \
  "v_mfma_f32_32x32x8_bf16 v[0:15], v[84:85], v[220:221], v[0:15]\n\t" \
  "v_exp_f32 v52, v52\n\t" \
  "v_exp_f32 v53, v53\n\t" \
  "v_exp_f32 v54, v54\n\t" \
  "v_exp_f32 v55, v55\n\t" \
  "s_waitcnt lgkmcnt(0)\n\t" \
  "v_smfmac_f32_32x32x32_bf16 v[136:151], v[176:179], v[32:39], v168\n\t" \
  "v_exp_f32 v56, v56\n\t" \
  "v_exp_f32 v57, v57\n\t" \
  "v_exp_f32 v58, v58\n\t" \
  "v_exp_f32 v59, v59\n\t" \
  "v_smfmac_f32_32x32x32_bf16 v[136:151], v[180:183], v[40:47], v168\n\t" \
  "v_exp_f32 v60, v60\n\t" \
  "v_exp_f32 v61, v61\n\t" \
  "v_exp_f32 v62, v62\n\t" \
  "v_exp_f32 v63, v63\n\t" \
  "v_mfma_f32_32x32x16_bf16 v[16:31], v[80:83], v[222:225], 0\n\t" \
  "v_exp_f32 v0, v0\n\t" \
  "v_exp_f32 v1, v1\n\t" \
  "v_exp_f32 v2, v2\n\t" \
  "v_exp_f32 v3, v3\n\t" \
  "v_mfma_f32_32x32x8_bf16 v[16:31], v[84:85], v[226:227], v[16:31]\n\t" \
  "ds_read_b128 v[80:83], v194 offset:17152\n\t" \
  "v_exp_f32 v4, v4\n\t" \
  "v_exp_f32 v5, v5\n\t" \
  "v_exp_f32 v6, v6\n\t" \
  "v_exp_f32 v7, v7\n\t" \
  "v_smfmac_f32_32x32x32_bf16 v[152:167], v[176:179], v[48:55], v168\n\t" \
  "ds_read_b128 v[176:179], v196 offset:16128\n\t" \
  "v_exp_f32 v8, v8\n\t" \
  "v_exp_f32 v9, v9\n\t" \
  "v_exp_f32 v10, v10\n\t" \
  "v_exp_f32 v11, v11\n\t" \
  "v_smfmac_f32_32x32x32_bf16 v[152:167], v[180:183], v[56:63], v168\n\t" \
  "ds_read_b128 v[180:183], v196 offset:16832\n\t" \
  "v_exp_f32 v12, v12\n\t" \
  "v_exp_f32 v13, v13\n\t" \
  "v_exp_f32 v14, v14\n\t" \
  "v_exp_f32 v15, v15\n\t" \
  "s_waitcnt lgkmcnt(2)\n\t" \
  "v_mfma_f32_32x32x16_bf16 v[32:47], v[80:83], v[216:219], 0\n\t" \
  "v_exp_f32 v16, v16\n\t" \
  "v_exp_f32 v17, v17\n\t" \
  "v_exp_f32 v18, v18\n\t" \
  "v_exp_f32 v19, v19\n\t" \
  "v_mfma_f32_32x32x8_bf16 v[32:47], v[86:87], v[220:221], v[32:47]\n\t" \
  "v_exp_f32 v20, v20\n\t" \
  "v_exp_f32 v21, v21\n\t" \
  "v_exp_f32 v22, v22\n\t" \
  "v_exp_f32 v23, v23\n\t" \
  "s_waitcnt lgkmcnt(0)\n\t" \
  "v_smfmac_f32_32x32x32_bf16 v[136:151], v[176:179], v[0:7], v168\n\t" \
  "v_exp_f32 v24, v24\n\t" \
  "v_exp_f32 v25, v25\n\t" \
  "v_exp_f32 v26, v26\n\t" \
  "v_exp_f32 v27, v27\n\t" \
  "v_smfmac_f32_32x32x32_bf16 v[136:151], v[180:183], v[8:15], v168\n\t" \
  "v_exp_f32 v28, v28\n\t" \
  "v_exp_f32 v29, v29\n\t" \
  "v_exp_f32 v30, v30\n\t" \
  "v_exp_f32 v31, v31\n\t" \
  "v_add_u32 v194, %[dl], v194\n\t" \
  "v_add_u32 v195, %[dl], v195\n\t" \
  "s_cmp_eq_u32 %[st], 7\n\t" \
  "s_cbranch_scc1 LNBa_%=\n\t" \
  "s_waitcnt vmcnt(0)\n\t" \
  "s_barrier\n\t" \
  "LNBa_%=:\n\t" \
  "v_mfma_f32_32x32x16_bf16 v[48:63], v[80:83], v[222:225], 0\n\t" \
  "v_exp_f32 v32, v32\n\t" \
  "v_exp_f32 v33, v33\n\t" \
  "v_exp_f32 v34, v34\n\t" \
  "v_exp_f32 v35, v35\n\t" \
  "v_mfma_f32_32x32x8_bf16 v[48:63], v[86:87], v[226:227], v[48:63]\n\t" \
  "ds_read_b128 v[80:83], v194 offset:0\n\t" \
  "ds_read_b128 v[84:87], v195 offset:0\n\t" \
  "v_exp_f32 v36, v36\n\t" \
  "v_exp_f32 v37, v37\n\t" \
  "v_exp_f32 v38, v38\n\t" \
  "v_exp_f32 v39, v39\n\t" \
  "v_smfmac_f32_32x32x32_bf16 v[152:167], v[176:179], v[16:23], v168\n\t" \
  "ds_read_b128 v[176:179], v196 offset:17536\n\t" \
  "v_exp_f32 v40, v40\n\t" \
  "v_exp_f32 v41, v41\n\t" \
  "v_exp_f32 v42, v42\n\t" \
  "v_exp_f32 v43, v43\n\t" \
  "v_smfmac_f32_32x32x32_bf16 v[152:167], v[180:183], v[24:31], v168\n\t" \
  "ds_read_b128 v[180:183], v196 offset:18240\n\t" \
  "v_exp_f32 v44, v44\n\t" \
  "v_exp_f32 v45, v45\n\t" \
  "v_exp_f32 v46, v46\n\t" \
  "v_exp_f32 v47, v47\n\t" \
  "s_waitcnt lgkmcnt(2)\n\t" \
  "v_mfma_f32_32x32x16_bf16 v[0:15], v[80:83], v[216:219], 0\n\t" \
  "v_add_u32 v196, %[dl], v196\n\t" \
  "v_exp_f32 v48, v48\n\t" \
  "v_exp_f32 v49, v49\n\t" \
  "v_exp_f32 v50, v50\n\t" \
  "v_exp_f32 v51, v51\n\t" \
  "v_mfma_f32_32x32x8_bf16 v[0:15], v[84:85], v[220:221], v[0:15]\n\t" \
  "v_exp_f32 v52, v52\n\t" \
  "v_exp_f32 v53, v53\n\t" \
  "v_exp_f32 v54, v54\n\t" \
  "v_exp_f32 v55, v55\n\t" \
  "s_waitcnt lgkmcnt(0)\n\t" \
  "v_smfmac_f32_32x32x32_bf16 v[136:151], v[176:179], v[32:39], v168\n\t" \
  "v_exp_f32 v56, v56\n\t" \
  "v_exp_f32 v57, v57\n\t" \
  "v_exp_f32 v58, v58\n\t" \
  "v_exp_f32 v59, v59\n\t" \
  "v_smfmac_f32_32x32x32_bf16 v[136:151], v[180:183], v[40:47], v168\n\t" \
  "v_exp_f32 v60, v60\n\t" \
  "v_exp_f32 v61, v61\n\t" \
  "v_exp_f32 v62, v62\n\t" \
  "v_exp_f32 v63, v63\n\t" \
  "s_add_u32 %[st], %[st], 1\n\t" \
  "s_cmp_lt_u32 %[st], 8\n\t" \
  "s_cbranch_scc1 LAa_%=\n\t" \
  "v_smfmac_f32_32x32x32_bf16 v[152:167], v[176:179], v[48:55], v168\n\t" \
  "v_smfmac_f32_32x32x32_bf16 v[152:167], v[180:183], v[56:63], v168\n\t" \
  "s_setprio 0\n\t" \
  "s_branch LEND_%=\n\t" \
  "LHa0_%=:\n\t" \
  "s_cmp_eq_u32 %[st], 1\n\t" \
  "s_cbranch_scc0 LIa0_%=\n\t" \
  "buffer_store_dwordx4 v[236:239], v232, %[rfl], %[so1] offen sc1\n\t" \
  "s_add_u32 %[tmp], %[so1], 40960\n\t" \
  "buffer_store_dwordx4 v[236:239], v232, %[rfl], %[tmp] offen sc1\n\t" \
  "buffer_store_dwordx2 v[236:237], v233, %[rfl], %[so2] offen sc1\n\t" \
  "s_branch LGa0_%=\n\t" \
  "LIa0_%=:\n\t" \
  "s_cmp_eq_u32 %[st], 2\n\t" \
  "s_cbranch_scc0 LKa0_%=\n\t" \
  "s_cmp_eq_u32 %[w1k], 0\n\t" \
  "s_cbranch_scc0 LGa0_%=\n\t" \
  "s_mov_b64 exec, 1\n\t" \
  "buffer_store_dwordx2 v[192:193], off, %[rfl], %[fo2] sc1\n\t" \
  "s_mov_b64 exec, -1\n\t" \
  "s_branch LGa0_%=\n\t" \
  "LKa0_%=:\n\t" \
  "s_cmp_eq_u32 %[st], 7\n\t" \
  "s_cbranch_scc0 LRa0_%=\n\t" \
  "s_barrier\n\t" \
  "buffer_load_dwordx2 v[190:191], v234, %[rfl], %[fg2] offen sc1\n\t" \
  "s_branch LRa0_%=\n\t" \
  "LHa1_%=:\n\t" \
  "s_cmp_eq_u32 %[st], 0\n\t" \
  "s_cbranch_scc0 LRa1_%=\n\t" \
  "s_waitcnt vmcnt(0)\n\t" \
  "s_barrier\n\t" \
  "v_add_u32 v192, 0x7F4A7C15, v192\n\t" \
  "v_add_u32 v193, 0x9E3779B9, v193\n\t" \
  "s_cmp_eq_u32 %[w1k], 0\n\t" \
  "s_cbranch_scc0 LRa1_%=\n\t" \
  "s_mov_b64 exec, 1\n\t" \
  "buffer_store_dwordx2 v[192:193], off, %[rfl], %[fo] sc1\n\t" \
  "s_mov_b64 exec, -1\n\t" \
  "s_branch LRa1_%=\n\t" \
  "LHa2_%=:\n\t" \
  "s_cmp_eq_u32 %[st], 0\n\t" \
  "s_cbranch_scc0 LRa2_%=\n\t" \
  "buffer_load_dwordx2 v[190:191], v188, %[rfl], %[fg] offen sc1\n\t" \
  "s_branch LRa2_%=\n\t" \
  "LHa3_%=:\n\t" \
  "s_cmp_eq_u32 %[st], 0\n\t" \
  "s_cbranch_scc0 LRa3_%=\n\t" \
  "s_mov_b32 %[kq], %[lim]\n\t" \
  "s_waitcnt vmcnt(0)\n\t" \
  "LSP_%=:\n\t" \
  "v_cmp_ne_u64 vcc, v[190:191], v[192:193]\n\t" \
  "s_nop 4\n\t" \
  "s_cbranch_vccz LOK_%=\n\t" \
  "s_sub_u32 %[kq], %[kq], 1\n\t" \
  "s_cbranch_scc1 LOK_%=\n\t" \
  "s_sleep 1\n\t" \
  "buffer_load_dwordx2 v[190:191], v188, %[rfl], %[fg] offen sc1\n\t" \
  "s_waitcnt vmcnt(0)\n\t" \
  "s_branch LSP_%=\n\t" \
  "LOK_%=:\n\t" \
  "s_mov_b64 vcc, -1\n\t" \
  "s_mov_b32 m0, %[mb]\n\t" \
  "s_mov_b32 %[tmp], %[sx]\n\t" \
  "buffer_load_dwordx4 v189, %[rkv], %[tmp] offen sc1 lds\n\t" \
  "buffer_load_dwordx4 v189, %[rkv], %[tmp] offen offset:1024 sc1 lds\n\t" \
  "buffer_load_dwordx4 v189, %[rkv], %[tmp] offen offset:2048 sc1 lds\n\t" \
  "buffer_load_dwordx4 v189, %[rkv], %[tmp] offen offset:3072 sc1 lds\n\t" \
  "s_add_u32 m0, %[mb], 4096\n\t" \
  "s_add_u32 %[tmp], %[sx], 4096\n\t" \
  "buffer_load_dwordx4 v189, %[rkv], %[tmp] offen sc1 lds\n\t" \
  "buffer_load_dwordx4 v189, %[rkv], %[tmp] offen offset:1024 sc1 lds\n\t" \
  "buffer_load_dwordx4 v189, %[rkv], %[tmp] offen offset:2048 sc1 lds\n\t" \
  "buffer_load_dwordx4 v189, %[rkv], %[tmp] offen offset:3072 sc1 lds\n\t" \
  "s_add_u32 m0, %[mb], 8192\n\t" \
  "s_add_u32 %[tmp], %[sx], 8192\n\t" \
  "buffer_load_dwordx4 v189, %[rkv], %[tmp] offen sc1 lds\n\t" \
  "buffer_load_dwordx4 v189, %[rkv], %[tmp] offen offset:1024 sc1 lds\n\t" \
  "buffer_load_dwordx4 v189, %[rkv], %[tmp] offen offset:1536 sc1 lds\n\t" \
  "s_branch LRa3_%=\n\t" \
  "LHI_%=:\n\t" \
  "s_setprio 1\n\t" \
  "LAb_%=:\n\t" \
  "s_add_u32 %[nxt], %[cur], 43008\n\t" \
  "s_cmp_eq_u32 %[cur], 86016\n\t" \
  "s_cselect_b32 %[nxt], 0, %[nxt]\n\t" \
  "s_sub_u32 %[dl], %[nxt], %[cur]\n\t" \
  "s_mov_b32 %[cur], %[nxt]\n\t" \
  "s_bitcmp1_b32 0x81, %[st]\n\t" \
  "s_cselect_b64 vcc, -1, 0\n\t" \
  "v_mfma_f32_32x32x16_bf16 v[16:31], v[80:83], v[222:225], 0\n\t" \
  "v_exp_f32 v0, v0\n\t" \
  "v_exp_f32 v1, v1\n\t" \
  "v_exp_f32 v2, v2\n\t" \
  "v_exp_f32 v3, v3\n\t" \
  "v_mfma_f32_32x32x8_bf16 v[16:31], v[84:85], v[226:227], v[16:31]\n\t" \
  "ds_read_b128 v[80:83], v194 offset:1024\n\t" \
  "v_exp_f32 v4, v4\n\t" \
  "v_exp_f32 v5, v5\n\t" \
  "v_exp_f32 v6, v6\n\t" \
  "v_exp_f32 v7, v7\n\t" \
  "v_smfmac_f32_32x32x32_bf16 v[152:167], v[176:179], v[48:55], v168\n\t" \
  "ds_read_b128 v[176:179], v196 offset:0\n\t" \
  "v_exp_f32 v8, v8\n\t" \
  "v_exp_f32 v9, v9\n\t" \
  "v_exp_f32 v10, v10\n\t" \
  "v_exp_f32 v11, v11\n\t" \
  "v_smfmac_f32_32x32x32_bf16 v[152:167], v[180:183], v[56:63], v168\n\t" \
  "ds_read_b128 v[180:183], v196 offset:704\n\t" \
  "v_exp_f32 v12, v12\n\t" \
  "v_exp_f32 v13, v13\n\t" \
  "v_exp_f32 v14, v14\n\t" \
  "v_exp_f32 v15, v15\n\t" \
  "s_waitcnt lgkmcnt(2)\n\t" \
  "v_mfma_f32_32x32x16_bf16 v[32:47], v[80:83], v[216:219], 0\n\t" \
  "v_exp_f32 v16, v16\n\t" \
  "v_exp_f32 v17, v17\n\t" \
  "v_exp_f32 v18, v18\n\t" \
  "v_exp_f32 v19, v19\n\t" \
  "v_mfma_f32_32x32x8_bf16 v[32:47], v[86:87], v[220:221], v[32:47]\n\t" \
  "v_exp_f32 v20, v20\n\t" \
  "v_exp_f32 v21, v21\n\t" \
  "v_exp_f32 v22, v22\n\t" \
  "v_exp_f32 v23, v23\n\t" \
  "s_bitcmp1_b32 0x82, %[st]\n\t" \
  "s_cbranch_scc1 LHb0_%=\n\t" \
  "LGb0_%=:\n\t" \
  "LRb0_%=:\n\t" \
  "s_waitcnt lgkmcnt(0)\n\t" \
  "v_smfmac_f32_32x32x32_bf16 v[136:151], v[176:179], v[0:7], v168\n\t" \
  "v_exp_f32 v24, v24\n\t" \
  "v_exp_f32 v25, v25\n\t" \
  "v_exp_f32 v26, v26\n\t" \
  "v_exp_f32 v27, v27\n\t" \
  "v_smfmac_f32_32x32x32_bf16 v[136:151], v[180:183], v[8:15], v168\n\t" \
  "v_exp_f32 v28, v28\n\t" \
  "v_exp_f32 v29, v29\n\t" \
  "v_exp_f32 v30, v30\n\t" \
  "v_exp_f32 v31, v31\n\t" \
  "v_mfma_f32_32x32x16_bf16 v[48:63], v[80:83], v[222:225], 0\n\t" \
  "v_exp_f32 v32, v32\n\t" \
  "v_exp_f32 v33, v33\n\t" \
  "v_exp_f32 v34, v34\n\t" \
  "v_exp_f32 v35, v35\n\t" \
  "v_mfma_f32_32x32x8_bf16 v[48:63], v[86:87], v[226:227], v[48:63]\n\t" \
  "ds_read_b128 v[80:83], v194 offset:5376\n\t" \
  "ds_read_b128 v[84:87], v195 offset:5376\n\t" \
  "v_exp_f32 v36, v36\n\t" \
  "v_exp_f32 v37, v37\n\t" \
  "v_exp_f32 v38, v38\n\t" \
  "v_exp_f32 v39, v39\n\t" \
  "v_smfmac_f32_32x32x32_bf16 v[152:167], v[176:179], v[16:23], v168\n\t" \
  "ds_read_b128 v[176:179], v196 offset:1408\n\t" \
  "v_exp_f32 v40, v40\n\t" \
  "v_exp_f32 v41, v41\n\t" \
  "v_exp_f32 v42, v42\n\t" \
  "v_exp_f32 v43, v43\n\t" \
  "v_smfmac_f32_32x32x32_bf16 v[152:167], v[180:183], v[24:31], v168\n\t" \
  "ds_read_b128 v[180:183], v196 offset:2112\n\t" \
  "v_exp_f32 v44, v44\n\t" \
  "v_exp_f32 v45, v45\n\t" \
  "v_exp_f32 v46, v46\n\t" \
  "v_exp_f32 v47, v47\n\t" \
  "s_waitcnt lgkmcnt(2)\n\t" \
  "v_mfma_f32_32x32x16_bf16 v[0:15], v[80:83], v[216:219], 0\n\t" \
  "v_exp_f32 v48, v48\n\t" \
  "v_exp_f32 v49, v49\n\t" \
  "v_exp_f32 v50, v50\n\t" \
  "v_exp_f32 v51, v51\n\t" \
  "v_mfma_f32_32x32x8_bf16 v[0:15], v[84:85], v[220:221], v[0:15]\n\t" \
  "v_exp_f32 v52, v52\n\t" \
  "v_exp_f32 v53, v53\n\t" \
  "v_exp_f32 v54, v54\n\t" \
  "v_exp_f32 v55, v55\n\t" \
  "s_cbranch_vccnz LHb1_%=\n\t" \
  "LGb1_%=:\n\t" \
  "LRb1_%=:\n\t" \
  "s_waitcnt lgkmcnt(0)\n\t" \
  "v_smfmac_f32_32x32x32_bf16 v[136:151], v[176:179], v[32:39], v168\n\t" \
  "v_exp_f32 v56, v56\n\t" \
  "v_exp_f32 v57, v57\n\t" \
  "v_exp_f32 v58, v58\n\t" \
  "v_exp_f32 v59, v59\n\t" \
  "v_smfmac_f32_32x32x32_bf16 v[136:151], v[180:183], v[40:47], v168\n\t" \
  "v_exp_f32 v60, v60\n\t" \
  "v_exp_f32 v61, v61\n\t" \
  "v_exp_f32 v62, v62\n\t" \
  "v_exp_f32 v63, v63\n\t" \
  "v_mfma_f32_32x32x16_bf16 v[16:31], v[80:83], v[222:225], 0\n\t" \
  "v_exp_f32 v0, v0\n\t" \
  "v_exp_f32 v1, v1\n\t" \
  "v_exp_f32 v2, v2\n\t" \
  "v_exp_f32 v3, v3\n\t" \
  "v_mfma_f32_32x32x8_bf16 v[16:31], v[84:85], v[226:227], v[16:31]\n\t" \
  "ds_read_b128 v[80:83], v194 offset:6400\n\t" \
  "v_exp_f32 v4, v4\n\t" \
  "v_exp_f32 v5, v5\n\t" \
  "v_exp_f32 v6, v6\n\t" \
  "v_exp_f32 v7, v7\n\t" \
  "v_smfmac_f32_32x32x32_bf16 v[152:167], v[176:179], v[48:55], v168\n\t" \
  "ds_read_b128 v[176:179], v196 offset:5376\n\t" \
  "v_exp_f32 v8, v8\n\t" \
  "v_exp_f32 v9, v9\n\t" \
  "v_exp_f32 v10, v10\n\t" \
  "v_exp_f32 v11, v11\n\t" \
  "v_smfmac_f32_32x32x32_bf16 v[152:167], v[180:183], v[56:63], v168\n\t" \
  "ds_read_b128 v[180:183], v196 offset:6080\n\t" \
  "v_exp_f32 v12, v12\n\t" \
  "v_exp_f32 v13, v13\n\t" \
  "v_exp_f32 v14, v14\n\t" \
  "v_exp_f32 v15, v15\n\t" \
  "s_waitcnt lgkmcnt(2)\n\t" \
  "v_mfma_f32_32x32x16_bf16 v[32:47], v[80:83], v[216:219], 0\n\t" \
  "v_exp_f32 v16, v16\n\t" \
  "v_exp_f32 v17, v17\n\t" \
  "v_exp_f32 v18, v18\n\t" \
  "v_exp_f32 v19, v19\n\t" \
  "v_mfma_f32_32x32x8_bf16 v[32:47], v[86:87], v[220:221], v[32:47]\n\t" \
  "v_exp_f32 v20, v20\n\t" \
  "v_exp_f32 v21, v21\n\t" \
  "v_exp_f32 v22, v22\n\t" \
  "v_exp_f32 v23, v23\n\t" \
  "s_cbranch_vccnz LHb2_%=\n\t" \
  "LGb2_%=:\n\t" \
  "LRb2_%=:\n\t" \
  "s_waitcnt lgkmcnt(0)\n\t" \
  "v_smfmac_f32_32x32x32_bf16 v[136:151], v[176:179], v[0:7], v168\n\t" \
  "v_exp_f32 v24, v24\n\t" \
  "v_exp_f32 v25, v25\n\t" \
  "v_exp_f32 v26, v26\n\t" \
  "v_exp_f32 v27, v27\n\t" \
  "v_smfmac_f32_32x32x32_bf16 v[136:151], v[180:183], v[8:15], v168\n\t" \
  "v_exp_f32 v28, v28\n\t" \
  "v_exp_f32 v29, v29\n\t" \
  "v_exp_f32 v30, v30\n\t" \
  "v_exp_f32 v31, v31\n\t" \
  "v_mfma_f32_32x32x16_bf16 v[48:63], v[80:83], v[222:225], 0\n\t" \
  "v_exp_f32 v32, v32\n\t" \
  "v_exp_f32 v33, v33\n\t" \
  "v_exp_f32 v34, v34\n\t" \
  "v_exp_f32 v35, v35\n\t" \
  "v_mfma_f32_32x32x8_bf16 v[48:63], v[86:87], v[226:227], v[48:63]\n\t" \
  "ds_read_b128 v[80:83], v194 offset:10752\n\t" \
  "ds_read_b128 v[84:87], v195 offset:10752\n\t" \
  "v_exp_f32 v36, v36\n\t" \
  "v_exp_f32 v37, v37\n\t" \
  "v_exp_f32 v38, v38\n\t" \
  "v_exp_f32 v39, v39\n\t" \
  "v_smfmac_f32_32x32x32_bf16 v[152:167], v[176:179], v[16:23], v168\n\t" \
  "ds_read_b128 v[176:179], v196 offset:6784\n\t" \
  "v_exp_f32 v40, v40\n\t" \
  "v_exp_f32 v41, v41\n\t" \
  "v_exp_f32 v42, v42\n\t" \
  "v_exp_f32 v43, v43\n\t" \
  "v_smfmac_f32_32x32x32_bf16 v[152:167], v[180:183], v[24:31], v168\n\t" \
  "ds_read_b128 v[180:183], v196 offset:7488\n\t" \
  "v_exp_f32 v44, v44\n\t" \
  "v_exp_f32 v45, v45\n\t" \
  "v_exp_f32 v46, v46\n\t" \
  "v_exp_f32 v47, v47\n\t" \
  "s_waitcnt lgkmcnt(2)\n\t" \
  "v_mfma_f32_32x32x16_bf16 v[0:15], v[80:83], v[216:219], 0\n\t" \
  "v_exp_f32 v48, v48\n\t" \
  "v_exp_f32 v49, v49\n\t" \
  "v_exp_f32 v50, v50\n\t" \
  "v_exp_f32 v51, v51\n\t" \
  "v_mfma_f32_32x32x8_bf16 v[0:15], v[84:85], v[220:221], v[0:15]\n\t" \
  "v_exp_f32 v52, v52\n\t" \
  "v_exp_f32 v53, v53\n\t" \
  "v_exp_f32 v54, v54\n\t" \
  "v_exp_f32 v55, v55\n\t" \
  "s_cbranch_vccnz LHb3_%=\n\t" \
  "LGb3_%=:\n\t" \
  "LRb3_%=:\n\t" \
  "s_waitcnt lgkmcnt(0)\n\t" \
  "v_smfmac_f32_32x32x32_bf16 v[136:151], v[176:179], v[32:39], v168\n\t" \
  "v_exp_f32 v56, v56\n\t" \
  "v_exp_f32 v57, v57\n\t" \
  "v_exp_f32 v58, v58\n\t" \
  "v_exp_f32 v59, v59\n\t" \
  "v_smfmac_f32_32x32x32_bf16 v[136:151], v[180:183], v[40:47], v168\n\t" \
  "v_exp_f32 v60, v60\n\t" \
  "v_exp_f32 v61, v61\n\t" \
  "v_exp_f32 v62, v62\n\t" \
  "v_exp_f32 v63, v63\n\t" \
  "v_mfma_f32_32x32x16_bf16 v[16:31], v[80:83], v[222:225], 0\n\t" \
  "v_exp_f32 v0, v0\n\t" \
  "v_exp_f32 v1, v1\n\t" \
  "v_exp_f32 v2, v2\n\t" \
  "v_exp_f32 v3, v3\n\t" \
  "v_mfma_f32_32x32x8_bf16 v[16:31], v[84:85], v[226:227], v[16:31]\n\t" \
  "ds_read_b128 v[80:83], v194 offset:11776\n\t" \
  "v_exp_f32 v4, v4\n\t" \
  "v_exp_f32 v5, v5\n\t" \
  "v_exp_f32 v6, v6\n\t" \
  "v_exp_f32 v7, v7\n\t" \
  "v_smfmac_f32_32x32x32_bf16 v[152:167], v[176:179], v[48:55], v168\n\t" \
  "ds_read_b128 v[176:179], v196 offset:10752\n\t" \
  "v_exp_f32 v8, v8\n\t" \
  "v_exp_f32 v9, v9\n\t" \
  "v_exp_f32 v10, v10\n\t" \
  "v_exp_f32 v11, v11\n\t" \
  "v_smfmac_f32_32x32x32_bf16 v[152:167], v[180:183], v[56:63], v168\n\t" \
  "ds_read_b128 v[180:183], v196 offset:11456\n\t" \
  "v_exp_f32 v12, v12\n\t" \
  "v_exp_f32 v13, v13\n\t" \
  "v_exp_f32 v14, v14\n\t" \
  "v_exp_f32 v15, v15\n\t" \
  "s_waitcnt lgkmcnt(2)\n\t" \
  "v_mfma_f32_32x32x16_bf16 v[32:47], v[80:83], v[216:219], 0\n\t" \
  "s_setprio 0\n\t" \
  "v_exp_f32 v16, v16\n\t" \
  "v_exp_f32 v17, v17\n\t" \
  "v_exp_f32 v18, v18\n\t" \
  "v_exp_f32 v19, v19\n\t" \
  "v_mfma_f32_32x32x8_bf16 v[32:47], v[86:87], v[220:221], v[32:47]\n\t" \
  "v_exp_f32 v20, v20\n\t" \
  "v_exp_f32 v21, v21\n\t" \
  "v_exp_f32 v22, v22\n\t" \
  "v_exp_f32 v23, v23\n\t" \
  "s_waitcnt lgkmcnt(0)\n\t" \
  "v_smfmac_f32_32x32x32_bf16 v[136:151], v[176:179], v[0:7], v168\n\t" \
  "v_exp_f32 v24, v24\n\t" \
  "v_exp_f32 v25, v25\n\t" \
  "v_exp_f32 v26, v26\n\t" \
  "v_exp_f32 v27, v27\n\t" \
  "v_smfmac_f32_32x32x32_bf16 v[136:151], v[180:183], v[8:15], v168\n\t" \
  "v_exp_f32 v28, v28\n\t" \
  "v_exp_f32 v29, v29\n\t" \
  "v_exp_f32 v30, v30\n\t" \
  "v_exp_f32 v31, v31\n\t" \
  "v_mfma_f32_32x32x16_bf16 v[48:63], v[80:83], v[222:225], 0\n\t" \
  "v_exp_f32 v32, v32\n\t" \
  "v_exp_f32 v33, v33\n\t" \
  "v_exp_f32 v34, v34\n\t" \
  "v_exp_f32 v35, v35\n\t" \
  "v_mfma_f32_32x32x8_bf16 v[48:63], v[86:87], v[226:227], v[48:63]\n\t" \
  "ds_read_b128 v[80:83], v194 offset:16128\n\t" \
  "ds_read_b128 v[84:87], v195 offset:16128\n\t" \
  "v_exp_f32 v36, v36\n\t" \
  "v_exp_f32 v37, v37\n\t" \
  "v_exp_f32 v38, v38\n\t" \
  "v_exp_f32 v39, v39\n\t" \
  "v_smfmac_f32_32x32x32_bf16 v[152:167], v[176:179], v[16:23], v168\n\t" \
  "ds_read_b128 v[176:179], v196 offset:12160\n\t" \
  "v_exp_f32 v40, v40\n\t" \
  "v_exp_f32 v41, v41\n\t" \
  "v_exp_f32 v42, v42\n\t" \
  "v_exp_f32 v43, v43\n\t" \
  "v_smfmac_f32_32x32x32_bf16 v[152:167], v[180:183], v[24:31], v168\n\t" \
  "ds_read_b128 v[180:183], v196 offset:12864\n\t" \
  "v_exp_f32 v44, v44\n\t" \
  "v_exp_f32 v45, v45\n\t" \
  "v_exp_f32 v46, v46\n\t" \
  "v_exp_f32 v47, v47\n\t" \
  "s_waitcnt lgkmcnt(2)\n\t" \
  "v_mfma_f32_32x32x16_bf16 v[0:15], v[80:83], v[216:219], 0\n\t" \
  "v_exp_f32 v48, v48\n\t" \
  "v_exp_f32 v49, v49\n\t" \
  "v_exp_f32 v50, v50\n\t" \
  "v_exp_f32 v51, v51\n\t" \
  "v_mfma_f32_32x32x8_bf16 v[0:15], v[84:85], v[220:221], v[0:15]\n\t" \
  "v_exp_f32 v52, v52\n\t" \
  "v_exp_f32 v53, v53\n\t" \
  "v_exp_f32 v54, v54\n\t" \
  "v_exp_f32 v55, v55\n\t" \
  "s_waitcnt lgkmcnt(0)\n\t" \
  "v_smfmac_f32_32x32x32_bf16 v[136:151], v[176:179], v[32:39], v168\n\t" \
  "v_exp_f32 v56, v56\n\t" \
  "v_exp_f32 v57, v57\n\t" \
  "v_exp_f32 v58, v58\n\t" \
  "v_exp_f32 v59, v59\n\t" \
  "v_smfmac_f32_32x32x32_bf16 v[136:151], v[180:183], v[40:47], v168\n\t" \
  "v_exp_f32 v60, v60\n\t" \
  "v_exp_f32 v61, v61\n\t" \
  "v_exp_f32 v62, v62\n\t" \
  "v_exp_f32 v63, v63\n\t" \
  "v_mfma_f32_32x32x16_bf16 v[16:31], v[80:83], v[222:225], 0\n\t" \
  "v_exp_f32 v0, v0\n\t" \
  "v_exp_f32 v1, v1\n\t" \
  "v_exp_f32 v2, v2\n\t" \
  "v_exp_f32 v3, v3\n\t" \
  "v_mfma_f32_32x32x8_bf16 v[16:31], v[84:85], v[226:227], v[16:31]\n\t" \
  "ds_read_b128 v[80:83], v194 offset:17152\n\t" \
  "v_exp_f32 v4, v4\n\t" \
  "v_exp_f32 v5, v5\n\t" \
  "v_exp_f32 v6, v6\n\t" \
  "v_exp_f32 v7, v7\n\t" \
  "v_smfmac_f32_32x32x32_bf16 v[152:167], v[176:179], v[48:55], v168\n\t" \
  "ds_read_b128 v[176:179], v196 offset:16128\n\t" \
  "v_exp_f32 v8, v8\n\t" \
  "v_exp_f32 v9, v9\n\t" \
  "v_exp_f32 v10, v10\n\t" \
  "v_exp_f32 v11, v11\n\t" \
  "v_smfmac_f32_32x32x32_bf16 v[152:167], v[180:183], v[56:63], v168\n\t" \
  "ds_read_b128 v[180:183], v196 offset:16832\n\t" \
  "v_exp_f32 v12, v12\n\t" \
  "v_exp_f32 v13, v13\n\t" \
  "v_exp_f32 v14, v14\n\t" \
  "v_exp_f32 v15, v15\n\t" \
  "s_waitcnt lgkmcnt(2)\n\t" \
  "v_mfma_f32_32x32x16_bf16 v[32:47], v[80:83], v[216:219], 0\n\t" \
  "v_exp_f32 v16, v16\n\t" \
  "v_exp_f32 v17, v17\n\t" \
  "v_exp_f32 v18, v18\n\t" \
  "v_exp_f32 v19, v19\n\t" \
  "v_mfma_f32_32x32x8_bf16 v[32:47], v[86:87], v[220:221], v[32:47]\n\t" \
  "v_exp_f32 v20, v20\n\t" \
  "v_exp_f32 v21, v21\n\t" \
  "v_exp_f32 v22, v22\n\t" \
  "v_exp_f32 v23, v23\n\t" \
  "s_waitcnt lgkmcnt(0)\n\t" \
  "v_smfmac_f32_32x32x32_bf16 v[136:151], v[176:179], v[0:7], v168\n\t" \
  "v_exp_f32 v24, v24\n\t" \
  "v_exp_f32 v25, v25\n\t" \
  "v_exp_f32 v26, v26\n\t" \
  "v_exp_f32 v27, v27\n\t" \
  "v_smfmac_f32_32x32x32_bf16 v[136:151], v[180:183], v[8:15], v168\n\t" \
  "v_exp_f32 v28, v28\n\t" \
  "v_exp_f32 v29, v29\n\t" \
  "v_exp_f32 v30, v30\n\t" \
  "v_exp_f32 v31, v31\n\t" \
  "v_add_u32 v194, %[dl], v194\n\t" \
  "v_add_u32 v195, %[dl], v195\n\t" \
  "s_cmp_eq_u32 %[st], 7\n\t" \
  "s_cbranch_scc1 LNBb_%=\n\t" \
  "s_waitcnt vmcnt(0)\n\t" \
  "s_barrier\n\t" \
  "LNBb_%=:\n\t" \
  "v_mfma_f32_32x32x16_bf16 v[48:63], v[80:83], v[222:225], 0\n\t" \
  "v_exp_f32 v32, v32\n\t" \
  "v_exp_f32 v33, v33\n\t" \
  "v_exp_f32 v34, v34\n\t" \
  "v_exp_f32 v35, v35\n\t" \
  "v_mfma_f32_32x32x8_bf16 v[48:63], v[86:87], v[226:227], v[48:63]\n\t" \
  "ds_read_b128 v[80:83], v194 offset:0\n\t" \
  "ds_read_b128 v[84:87], v195 offset:0\n\t" \
  "v_exp_f32 v36, v36\n\t" \
  "v_exp_f32 v37, v37\n\t" \
  "v_exp_f32 v38, v38\n\t" \
  "v_exp_f32 v39, v39\n\t" \
  "v_smfmac_f32_32x32x32_bf16 v[152:167], v[176:179], v[16:23], v168\n\t" \
  "ds_read_b128 v[176:179], v196 offset:17536\n\t" \
  "v_exp_f32 v40, v40\n\t" \
  "v_exp_f32 v41, v41\n\t" \
  "v_exp_f32 v42, v42\n\t" \
  "v_exp_f32 v43, v43\n\t" \
  "v_smfmac_f32_32x32x32_bf16 v[152:167], v[180:183], v[24:31], v168\n\t" \
  "ds_read_b128 v[180:183], v196 offset:18240\n\t" \
  "v_exp_f32 v44, v44\n\t" \
  "v_exp_f32 v45, v45\n\t" \
  "v_exp_f32 v46, v46\n\t" \
  "v_exp_f32 v47, v47\n\t" \
  "s_waitcnt lgkmcnt(2)\n\t" \
  "v_mfma_f32_32x32x16_bf16 v[0:15], v[80:83], v[216:219], 0\n\t" \
  "v_add_u32 v196, %[dl], v196\n\t" \
  "s_setprio 1\n\t" \
  "v_exp_f32 v48, v48\n\t" \
  "v_exp_f32 v49, v49\n\t" \
  "v_exp_f32 v50, v50\n\t" \
  "v_exp_f32 v51, v51\n\t" \
  "v_mfma_f32_32x32x8_bf16 v[0:15], v[84:85], v[220:221], v[0:15]\n\t" \
  "v_exp_f32 v52, v52\n\t" \
  "v_exp_f32 v53, v53\n\t" \
  "v_exp_f32 v54, v54\n\t" \
  "v_exp_f32 v55, v55\n\t" \
  "s_waitcnt lgkmcnt(0)\n\t" \
  "v_smfmac_f32_32x32x32_bf16 v[136:151], v[176:179], v[32:39], v168\n\t" \
  "v_exp_f32 v56, v56\n\t" \
  "v_exp_f32 v57, v57\n\t" \
  "v_exp_f32 v58, v58\n\t" \
  "v_exp_f32 v59, v59\n\t" \
  "v_smfmac_f32_32x32x32_bf16 v[136:151], v[180:183], v[40:47], v168\n\t" \
  "v_exp_f32 v60, v60\n\t" \
  "v_exp_f32 v61, v61\n\t" \
  "v_exp_f32 v62, v62\n\t" \
  "v_exp_f32 v63, v63\n\t" \
  "s_add_u32 %[st], %[st], 1\n\t" \
  "s_cmp_lt_u32 %[st], 8\n\t" \
  "s_cbranch_scc1 LAb_%=\n\t" \
  "v_smfmac_f32_32x32x32_bf16 v[152:167], v[176:179], v[48:55], v168\n\t" \
  "v_smfmac_f32_32x32x32_bf16 v[152:167], v[180:183], v[56:63], v168\n\t" \
  "s_setprio 0\n\t" \
  "s_branch LEND_%=\n\t" \
  "LHb0_%=:\n\t" \
  "s_cmp_eq_u32 %[st], 1\n\t" \
  "s_cbranch_scc0 LIb0_%=\n\t" \
  "buffer_store_dwordx4 v[236:239], v232, %[rfl], %[so1] offen sc1\n\t" \
  "s_add_u32 %[tmp], %[so1], 40960\n\t" \
  "buffer_store_dwordx4 v[236:239], v232, %[rfl], %[tmp] offen sc1\n\t" \
  "buffer_store_dwordx2 v[236:237], v233, %[rfl], %[so2] offen sc1\n\t" \
  "s_branch LGb0_%=\n\t" \
  "LIb0_%=:\n\t" \
  "s_cmp_eq_u32 %[st], 7\n\t" \
  "s_cbranch_scc0 LRb0_%=\n\t" \
  "s_barrier\n\t" \
  "buffer_load_dwordx2 v[190:191], v234, %[rfl], %[fg2] offen sc1\n\t" \
  "s_branch LRb0_%=\n\t" \
  "LHb1_%=:\n\t" \
  "s_cmp_eq_u32 %[st], 0\n\t" \
  "s_cbranch_scc0 LSb1_%=\n\t" \
  "s_waitcnt vmcnt(0)\n\t" \
  "s_barrier\n\t" \
  "v_add_u32 v192, 0x7F4A7C15, v192\n\t" \
  "v_add_u32 v193, 0x9E3779B9, v193\n\t" \
  "s_branch LRb1_%=\n\t" \
  "LSb1_%=:\n\t" \
  "s_add_u32 m0, %[lw], 43008\n\t" \
  "s_add_u32 %[tmp], %[sa1], 0\n\t" \
  "buffer_load_dwordx4 v184, %[rwo], %[tmp] offen lds\n\t" \
  "s_add_u32 m0, %[lw], 38912\n\t" \
  "s_add_u32 %[tmp], %[sa1], 0xffff3800\n\t" \
  "buffer_load_dwordx4 v184, %[rwo], %[tmp] offen lds\n\t" \
  "s_add_u32 m0, %[lw], 51200\n\t" \
  "s_add_u32 %[tmp], %[sa1], 102400\n\t" \
  "buffer_load_dwordx4 v184, %[rwo], %[tmp] offen lds\n\t" \
  "s_add_u32 m0, %[lw], 47104\n\t" \
  "s_add_u32 %[tmp], %[sa1], 51200\n\t" \
  "buffer_load_dwordx4 v184, %[rwo], %[tmp] offen lds\n\t" \
  "s_add_u32 m0, %[lw], 59392\n\t" \
  "s_add_u32 %[tmp], %[sa1], 204800\n\t" \
  "buffer_load_dwordx4 v184, %[rwo], %[tmp] offen lds\n\t" \
  "s_add_u32 m0, %[lw], 55296\n\t" \
  "s_add_u32 %[tmp], %[sa1], 153600\n\t" \
  "buffer_load_dwordx4 v184, %[rwo], %[tmp] offen lds\n\t" \
  "s_add_u32 m0, %[lw], 67584\n\t" \
  "s_add_u32 %[tmp], %[sa1], 307200\n\t" \
  "buffer_load_dwordx4 v184, %[rwo], %[tmp] offen lds\n\t" \
  "s_add_u32 m0, %[lw], 63488\n\t" \
  "s_add_u32 %[tmp], %[sa1], 256000\n\t" \
  "buffer_load_dwordx4 v184, %[rwo], %[tmp] offen lds\n\t" \
  "s_add_u32 m0, %[lw], 75776\n\t" \
  "s_add_u32 %[tmp], %[sa1], 409600\n\t" \
  "buffer_load_dwordx4 v184, %[rwo], %[tmp] offen lds\n\t" \
  "s_add_u32 m0, %[lw], 71680\n\t" \
  "s_add_u32 %[tmp], %[sa1], 358400\n\t" \
  "buffer_load_dwordx4 v184, %[rwo], %[tmp] offen lds\n\t" \
  "s_branch LRb1_%=\n\t" \
  "LHb2_%=:\n\t" \
  "s_cmp_eq_u32 %[st], 7\n\t" \
  "s_cbranch_scc0 LRb2_%=\n\t" \
  "s_add_u32 m0, %[lw], 83968\n\t" \
  "s_add_u32 %[tmp], %[sa1], 512000\n\t" \
  "buffer_load_dwordx4 v184, %[rwo], %[tmp] offen lds\n\t" \
  "s_add_u32 m0, %[lw], 79872\n\t" \
  "s_add_u32 %[tmp], %[sa1], 460800\n\t" \
  "buffer_load_dwordx4 v184, %[rwo], %[tmp] offen lds\n\t" \
  "s_add_u32 m0, %[lw], 92160\n\t" \
  "s_add_u32 %[tmp], %[sa1], 614400\n\t" \
  "buffer_load_dwordx4 v184, %[rwo], %[tmp] offen lds\n\t" \
  "s_add_u32 m0, %[lw], 88064\n\t" \
  "s_add_u32 %[tmp], %[sa1], 563200\n\t" \
  "buffer_load_dwordx4 v184, %[rwo], %[tmp] offen lds\n\t" \
  "s_add_u32 m0, %[lw], 100352\n\t" \
  "s_add_u32 %[tmp], %[sa1], 716800\n\t" \
  "buffer_load_dwordx4 v184, %[rwo], %[tmp] offen lds\n\t" \
  "s_add_u32 m0, %[lw], 96256\n\t" \
  "s_add_u32 %[tmp], %[sa1], 665600\n\t" \
  "buffer_load_dwordx4 v184, %[rwo], %[tmp] offen lds\n\t" \
  "s_add_u32 m0, %[lw], 108544\n\t" \
  "s_add_u32 %[tmp], %[sa2], 0\n\t" \
  "buffer_load_dwordx4 v185, %[rwo], %[tmp] offen lds\n\t" \
  "s_add_u32 m0, %[lw], 104448\n\t" \
  "s_add_u32 %[tmp], %[sa2], 0xffff3800\n\t" \
  "buffer_load_dwordx4 v185, %[rwo], %[tmp] offen lds\n\t" \
  "s_branch LRb2_%=\n\t" \
  "LHb3_%=:\n\t" \
  "s_cmp_eq_u32 %[st], 7\n\t" \
  "s_cbranch_scc0 LRb3_%=\n\t" \
  "s_add_u32 m0, %[lw], 116736\n\t" \
  "s_add_u32 %[tmp], %[sa2], 409600\n\t" \
  "buffer_load_dwordx4 v185, %[rwo], %[tmp] offen lds\n\t" \
  "s_add_u32 m0, %[lw], 112640\n\t" \
  "s_add_u32 %[tmp], %[sa2], 358400\n\t" \
  "buffer_load_dwordx4 v185, %[rwo], %[tmp] offen lds\n\t" \
  "s_add_u32 m0, %[lw], 124928\n\t" \
  "s_add_u32 %[tmp], %[sb1], 0\n\t" \
  "buffer_load_dwordx4 v186, %[rwo], %[tmp] offen lds\n\t" \
  "s_add_u32 m0, %[lw], 120832\n\t" \
  "s_add_u32 %[tmp], %[sb1], 0xfffce000\n\t" \
  "buffer_load_dwordx4 v186, %[rwo], %[tmp] offen lds\n\t" \
  "s_add_u32 m0, %[lw], 133120\n\t" \
  "s_add_u32 %[tmp], %[sb1], 409600\n\t" \
  "buffer_load_dwordx4 v186, %[rwo], %[tmp] offen lds\n\t" \
  "s_add_u32 m0, %[lw], 129024\n\t" \
  "s_add_u32 %[tmp], %[sb1], 204800\n\t" \
  "buffer_load_dwordx4 v186, %[rwo], %[tmp] offen lds\n\t" \
  "s_add_u32 m0, %[lw], 137216\n\t" \
  "s_mov_b32 %[tmp], %[sb2]\n\t" \
  "buffer_load_dwordx4 v187, %[rwo], %[tmp] offen lds\n\t" \
  "s_branch LRb3_%=\n\t" \
  "LEND_%=:\n\t" \
  "s_waitcnt vmcnt(0)\n\t" \
  "s_nop 7\n\t" \
  "s_nop 7\n\t"
#define P3_ASM \
  "s_add_u32 %[a1], %[a0], 4096\n\t" \
  "s_add_u32 %[b1], %[b0], 4096\n\t" \
  "s_mov_b32 %[cnt], %[lim]\n\t" \
  "s_add_u32 %[a2], %[a0], 8192\n\t" \
  "s_add_u32 %[b2], %[b0], 8192\n\t" \
  "v_lshrrev_b32 v174, 4, v165\n\t" \
  "v_and_b32 v175, 48, v174\n\t" \
  "v_lshl_add_u32 v160, v175, 6, %[la]\n\t" \
  "v_and_b32 v175, 12, v174\n\t" \
  "v_lshl_add_u32 v160, v175, 5, v160\n\t" \
  "v_and_b32 v175, 3, v174\n\t" \
  "v_lshl_add_u32 v160, v175, 3, v160\n\t" \
  "v_and_b32 v175, 24, v174\n\t" \
  "v_lshl_add_u32 v160, v175, 2, v160\n\t" \
  "v_xor_b32 v161, 32, v160\n\t" \
  "v_xor_b32 v162, 64, v160\n\t" \
  "v_xor_b32 v163, 0x60, v160\n\t" \
  "v_and_b32 v175, 32, v174\n\t" \
  "v_lshl_add_u32 v164, v175, 4, %[lb4]\n\t" \
  "v_and_b32 v175, 31, v174\n\t" \
  "v_lshl_add_u32 v164, v175, 3, v164\n\t" \
  "s_barrier\n\t" \
  "ds_read_b64_tr_b16 v[120:121], v160 offset:0\n\t" \
  "ds_read_b64_tr_b16 v[122:123], v160 offset:512\n\t" \
  "ds_read_b64_tr_b16 v[124:125], v161 offset:0\n\t" \
  "ds_read_b64_tr_b16 v[126:127], v161 offset:512\n\t" \
  "ds_read_b64_tr_b16 v[128:129], v162 offset:0\n\t" \
  "ds_read_b64_tr_b16 v[130:131], v162 offset:512\n\t" \
  "ds_read_b64_tr_b16 v[132:133], v163 offset:0\n\t" \
  "ds_read_b64_tr_b16 v[134:135], v163 offset:512\n\t" \
  "ds_read_b64_tr_b16 v[136:137], v164 offset:0\n\t" \
  "ds_read_b64_tr_b16 v[138:139], v164 offset:256\n\t" \
  "buffer_load_dwordx4 v[0:3], v165, %[ro], %[a0] offen sc1\n\t" \
  "buffer_load_dwordx4 v[4:7], v165, %[ro], %[b0] offen sc1\n\t" \
  "buffer_load_dwordx4 v[8:11], v165, %[ro], %[a0] offen offset:2048 sc1\n\t" \
  "buffer_load_dwordx4 v[12:15], v165, %[ro], %[b0] offen offset:2048 sc1\n\t" \
  "v_add_u32 v170, %[lm], v165\n\t" \
  "v_add_u32 v171, %[lo], v165\n\t" \
  "v_and_b32 v175, 15, v174\n\t" \
  "v_mul_u32_u24 v173, 0xa00, v175\n\t" \
  "v_and_b32 v175, 16, v174\n\t" \
  "v_lshl_add_u32 v172, v175, 1, v173\n\t" \
  "v_and_b32 v175, 32, v174\n\t" \
  "v_lshrrev_b32 v175, 1, v175\n\t" \
  "v_add_u32 v172, v172, v175\n\t" \
  "v_and_b32 v175, 48, v174\n\t" \
  "v_lshrrev_b32 v175, 1, v175\n\t" \
  "v_add_u32 v173, v173, v175\n\t" \
  "s_waitcnt vmcnt(2)\n\t" \
  "v_max3_u32 v168, v0, v1, v2\n\t" \
  "v_max3_u32 v169, v3, v4, v5\n\t" \
  "v_max3_u32 v168, v168, v6, v7\n\t" \
  "v_max_u32 v168, v168, v169\n\t" \
  "v_cmp_eq_u32 vcc, -1, v168\n\t" \
  "s_cbranch_vccnz LS0_%=\n\t" \
  "LK0_%=:\n\t" \
  "s_waitcnt lgkmcnt(0)\n\t" \
  "v_mfma_f32_16x16x32_bf16 v[80:83], v[120:123], v[0:3], 0\n\t" \
  "buffer_load_dwordx4 v[16:19], v165, %[ro], %[a1] offen sc1\n\t" \
  "ds_read_b64_tr_b16 v[140:141], v160 offset:8192\n\t" \
  "v_mfma_f32_16x16x32_bf16 v[100:103], v[120:123], v[4:7], 0\n\t" \
  "buffer_load_dwordx4 v[20:23], v165, %[ro], %[b1] offen sc1\n\t" \
  "ds_read_b64_tr_b16 v[142:143], v160 offset:8704\n\t" \
  "v_mfma_f32_16x16x32_bf16 v[84:87], v[124:127], v[0:3], 0\n\t" \
  "ds_read_b64_tr_b16 v[144:145], v161 offset:8192\n\t" \
  "v_mfma_f32_16x16x32_bf16 v[104:107], v[124:127], v[4:7], 0\n\t" \
  "ds_read_b64_tr_b16 v[146:147], v161 offset:8704\n\t" \
  "v_mfma_f32_16x16x32_bf16 v[88:91], v[128:131], v[0:3], 0\n\t" \
  "ds_read_b64_tr_b16 v[148:149], v162 offset:8192\n\t" \
  "v_mfma_f32_16x16x32_bf16 v[108:111], v[128:131], v[4:7], 0\n\t" \
  "ds_read_b64_tr_b16 v[150:151], v162 offset:8704\n\t" \
  "s_waitcnt vmcnt(2)\n\t" \
  "v_max3_u32 v168, v8, v9, v10\n\t" \
  "v_mfma_f32_16x16x32_bf16 v[92:95], v[132:135], v[0:3], 0\n\t" \
  "ds_read_b64_tr_b16 v[152:153], v163 offset:8192\n\t" \
  "v_max3_u32 v169, v11, v12, v13\n\t" \
  "v_mfma_f32_16x16x32_bf16 v[112:115], v[132:135], v[4:7], 0\n\t" \
  "ds_read_b64_tr_b16 v[154:155], v163 offset:8704\n\t" \
  "v_max3_u32 v168, v168, v14, v15\n\t" \
  "v_mfma_f32_16x16x32_bf16 v[96:99], v[136:139], v[0:3], 0\n\t" \
  "ds_read_b64_tr_b16 v[156:157], v164 offset:2048\n\t" \
  "v_max_u32 v168, v168, v169\n\t" \
  "v_mfma_f32_16x16x32_bf16 v[116:119], v[136:139], v[4:7], 0\n\t" \
  "ds_read_b64_tr_b16 v[158:159], v164 offset:2304\n\t" \
  "v_cmp_eq_u32 vcc, -1, v168\n\t" \
  "s_cbranch_vccnz LS1_%=\n\t" \
  "LK1_%=:\n\t" \
  "s_waitcnt lgkmcnt(0)\n\t" \
  "v_mfma_f32_16x16x32_bf16 v[80:83], v[140:143], v[8:11], v[80:83]\n\t" \
  "buffer_load_dwordx4 v[24:27], v165, %[ro], %[a1] offen offset:2048 sc1\n\t" \
  "ds_read_b64_tr_b16 v[120:121], v160 offset:16384\n\t" \
  "v_mfma_f32_16x16x32_bf16 v[100:103], v[140:143], v[12:15], v[100:103]\n\t" \
  "buffer_load_dwordx4 v[28:31], v165, %[ro], %[b1] offen offset:2048 sc1\n\t" \
  "ds_read_b64_tr_b16 v[122:123], v160 offset:16896\n\t" \
  "v_mfma_f32_16x16x32_bf16 v[84:87], v[144:147], v[8:11], v[84:87]\n\t" \
  "ds_read_b64_tr_b16 v[124:125], v161 offset:16384\n\t" \
  "v_mfma_f32_16x16x32_bf16 v[104:107], v[144:147], v[12:15], v[104:107]\n\t" \
  "ds_read_b64_tr_b16 v[126:127], v161 offset:16896\n\t" \
  "v_mfma_f32_16x16x32_bf16 v[88:91], v[148:151], v[8:11], v[88:91]\n\t" \
  "ds_read_b64_tr_b16 v[128:129], v162 offset:16384\n\t" \
  "v_mfma_f32_16x16x32_bf16 v[108:111], v[148:151], v[12:15], v[108:111]\n\t" \
  "ds_read_b64_tr_b16 v[130:131], v162 offset:16896\n\t" \
  "s_waitcnt vmcnt(2)\n\t" \
  "v_max3_u32 v168, v16, v17, v18\n\t" \
  "v_mfma_f32_16x16x32_bf16 v[92:95], v[152:155], v[8:11], v[92:95]\n\t" \
  "ds_read_b64_tr_b16 v[132:133], v163 offset:16384\n\t" \
  "v_max3_u32 v169, v19, v20, v21\n\t" \
  "v_mfma_f32_16x16x32_bf16 v[112:115], v[152:155], v[12:15], v[112:115]\n\t" \
  "ds_read_b64_tr_b16 v[134:135], v163 offset:16896\n\t" \
  "v_max3_u32 v168, v168, v22, v23\n\t" \
  "v_mfma_f32_16x16x32_bf16 v[96:99], v[156:159], v[8:11], v[96:99]\n\t" \
  "ds_read_b64_tr_b16 v[136:137], v164 offset:4096\n\t" \
  "v_max_u32 v168, v168, v169\n\t" \
  "v_mfma_f32_16x16x32_bf16 v[116:119], v[156:159], v[12:15], v[116:119]\n\t" \
  "ds_read_b64_tr_b16 v[138:139], v164 offset:4352\n\t" \
  "v_cmp_eq_u32 vcc, -1, v168\n\t" \
  "s_cbranch_vccnz LS2_%=\n\t" \
  "LK2_%=:\n\t" \
  "s_waitcnt lgkmcnt(0)\n\t" \
  "v_mfma_f32_16x16x32_bf16 v[80:83], v[120:123], v[16:19], v[80:83]\n\t" \
  "buffer_load_dwordx4 v[32:35], v165, %[ro], %[a2] offen sc1\n\t" \
  "ds_read_b64_tr_b16 v[140:141], v160 offset:24576\n\t" \
  "v_mfma_f32_16x16x32_bf16 v[100:103], v[120:123], v[20:23], v[100:103]\n\t" \
  "buffer_load_dwordx4 v[36:39], v165, %[ro], %[b2] offen sc1\n\t" \
  "ds_read_b64_tr_b16 v[142:143], v160 offset:25088\n\t" \
  "v_mfma_f32_16x16x32_bf16 v[84:87], v[124:127], v[16:19], v[84:87]\n\t" \
  "ds_read_b64_tr_b16 v[144:145], v161 offset:24576\n\t" \
  "v_mfma_f32_16x16x32_bf16 v[104:107], v[124:127], v[20:23], v[104:107]\n\t" \
  "ds_read_b64_tr_b16 v[146:147], v161 offset:25088\n\t" \
  "v_mfma_f32_16x16x32_bf16 v[88:91], v[128:131], v[16:19], v[88:91]\n\t" \
  "ds_read_b64_tr_b16 v[148:149], v162 offset:24576\n\t" \
  "v_mfma_f32_16x16x32_bf16 v[108:111], v[128:131], v[20:23], v[108:111]\n\t" \
  "ds_read_b64_tr_b16 v[150:151], v162 offset:25088\n\t" \
  "s_waitcnt vmcnt(2)\n\t" \
  "v_max3_u32 v168, v24, v25, v26\n\t" \
  "v_mfma_f32_16x16x32_bf16 v[92:95], v[132:135], v[16:19], v[92:95]\n\t" \
  "ds_read_b64_tr_b16 v[152:153], v163 offset:24576\n\t" \
  "v_max3_u32 v169, v27, v28, v29\n\t" \
  "v_mfma_f32_16x16x32_bf16 v[112:115], v[132:135], v[20:23], v[112:115]\n\t" \
  "ds_read_b64_tr_b16 v[154:155], v163 offset:25088\n\t" \
  "v_max3_u32 v168, v168, v30, v31\n\t" \
  "v_mfma_f32_16x16x32_bf16 v[96:99], v[136:139], v[16:19], v[96:99]\n\t" \
  "ds_read_b64_tr_b16 v[156:157], v164 offset:6144\n\t" \
  "v_max_u32 v168, v168, v169\n\t" \
  "v_mfma_f32_16x16x32_bf16 v[116:119], v[136:139], v[20:23], v[116:119]\n\t" \
  "ds_read_b64_tr_b16 v[158:159], v164 offset:6400\n\t" \
  "v_cmp_eq_u32 vcc, -1, v168\n\t" \
  "s_cbranch_vccnz LS3_%=\n\t" \
  "LK3_%=:\n\t" \
  "s_waitcnt lgkmcnt(0)\n\t" \
  "v_mfma_f32_16x16x32_bf16 v[80:83], v[140:143], v[24:27], v[80:83]\n\t" \
  "buffer_load_dwordx4 v[40:43], v165, %[ro], %[a0] offen offset:1024 sc1\n\t" \
  "ds_read_b64_tr_b16 v[120:121], v160 offset:32768\n\t" \
  "v_mfma_f32_16x16x32_bf16 v[100:103], v[140:143], v[28:31], v[100:103]\n\t" \
  "buffer_load_dwordx4 v[44:47], v165, %[ro], %[b0] offen offset:1024 sc1\n\t" \
  "ds_read_b64_tr_b16 v[122:123], v160 offset:33280\n\t" \
  "v_mfma_f32_16x16x32_bf16 v[84:87], v[144:147], v[24:27], v[84:87]\n\t" \
  "ds_read_b64_tr_b16 v[124:125], v161 offset:32768\n\t" \
  "v_mfma_f32_16x16x32_bf16 v[104:107], v[144:147], v[28:31], v[104:107]\n\t" \
  "ds_read_b64_tr_b16 v[126:127], v161 offset:33280\n\t" \
  "v_mfma_f32_16x16x32_bf16 v[88:91], v[148:151], v[24:27], v[88:91]\n\t" \
  "ds_read_b64_tr_b16 v[128:129], v162 offset:32768\n\t" \
  "v_mfma_f32_16x16x32_bf16 v[108:111], v[148:151], v[28:31], v[108:111]\n\t" \
  "ds_read_b64_tr_b16 v[130:131], v162 offset:33280\n\t" \
  "s_waitcnt vmcnt(2)\n\t" \
  "v_max3_u32 v168, v32, v33, v34\n\t" \
  "v_mfma_f32_16x16x32_bf16 v[92:95], v[152:155], v[24:27], v[92:95]\n\t" \
  "ds_read_b64_tr_b16 v[132:133], v163 offset:32768\n\t" \
  "v_max3_u32 v169, v35, v36, v37\n\t" \
  "v_mfma_f32_16x16x32_bf16 v[112:115], v[152:155], v[28:31], v[112:115]\n\t" \
  "ds_read_b64_tr_b16 v[134:135], v163 offset:33280\n\t" \
  "v_max3_u32 v168, v168, v38, v39\n\t" \
  "v_mfma_f32_16x16x32_bf16 v[96:99], v[156:159], v[24:27], v[96:99]\n\t" \
  "ds_read_b64_tr_b16 v[136:137], v164 offset:8192\n\t" \
  "v_max_u32 v168, v168, v169\n\t" \
  "v_mfma_f32_16x16x32_bf16 v[116:119], v[156:159], v[28:31], v[116:119]\n\t" \
  "ds_read_b64_tr_b16 v[138:139], v164 offset:8448\n\t" \
  "v_cmp_eq_u32 vcc, -1, v168\n\t" \
  "s_cbranch_vccnz LS4_%=\n\t" \
  "LK4_%=:\n\t" \
  "s_waitcnt lgkmcnt(0)\n\t" \
  "v_mfma_f32_16x16x32_bf16 v[80:83], v[120:123], v[32:35], v[80:83]\n\t" \
  "buffer_load_dwordx4 v[48:51], v165, %[ro], %[a0] offen offset:3072 sc1\n\t" \
  "ds_read_b64_tr_b16 v[140:141], v160 offset:4096\n\t" \
  "v_mfma_f32_16x16x32_bf16 v[100:103], v[120:123], v[36:39], v[100:103]\n\t" \
  "buffer_load_dwordx4 v[52:55], v165, %[ro], %[b0] offen offset:3072 sc1\n\t" \
  "ds_read_b64_tr_b16 v[142:143], v160 offset:4608\n\t" \
  "v_mfma_f32_16x16x32_bf16 v[84:87], v[124:127], v[32:35], v[84:87]\n\t" \
  "ds_read_b64_tr_b16 v[144:145], v161 offset:4096\n\t" \
  "v_mfma_f32_16x16x32_bf16 v[104:107], v[124:127], v[36:39], v[104:107]\n\t" \
  "ds_read_b64_tr_b16 v[146:147], v161 offset:4608\n\t" \
  "v_mfma_f32_16x16x32_bf16 v[88:91], v[128:131], v[32:35], v[88:91]\n\t" \
  "ds_read_b64_tr_b16 v[148:149], v162 offset:4096\n\t" \
  "v_mfma_f32_16x16x32_bf16 v[108:111], v[128:131], v[36:39], v[108:111]\n\t" \
  "ds_read_b64_tr_b16 v[150:151], v162 offset:4608\n\t" \
  "s_waitcnt vmcnt(2)\n\t" \
  "v_max3_u32 v168, v40, v41, v42\n\t" \
  "v_mfma_f32_16x16x32_bf16 v[92:95], v[132:135], v[32:35], v[92:95]\n\t" \
  "ds_read_b64_tr_b16 v[152:153], v163 offset:4096\n\t" \
  "v_max3_u32 v169, v43, v44, v45\n\t" \
  "v_mfma_f32_16x16x32_bf16 v[112:115], v[132:135], v[36:39], v[112:115]\n\t" \
  "ds_read_b64_tr_b16 v[154:155], v163 offset:4608\n\t" \
  "v_max3_u32 v168, v168, v46, v47\n\t" \
  "v_mfma_f32_16x16x32_bf16 v[96:99], v[136:139], v[32:35], v[96:99]\n\t" \
  "ds_read_b64_tr_b16 v[156:157], v164 offset:1024\n\t" \
  "v_max_u32 v168, v168, v169\n\t" \
  "v_mfma_f32_16x16x32_bf16 v[116:119], v[136:139], v[36:39], v[116:119]\n\t" \
  "ds_read_b64_tr_b16 v[158:159], v164 offset:1280\n\t" \
  "v_cmp_eq_u32 vcc, -1, v168\n\t" \
  "s_cbranch_vccnz LS5_%=\n\t" \
  "LK5_%=:\n\t" \
  "s_waitcnt lgkmcnt(0)\n\t" \
  "v_mfma_f32_16x16x32_bf16 v[80:83], v[140:143], v[40:43], v[80:83]\n\t" \
  "buffer_load_dwordx4 v[56:59], v165, %[ro], %[a1] offen offset:1024 sc1\n\t" \
  "ds_read_b64_tr_b16 v[120:121], v160 offset:12288\n\t" \
  "v_mfma_f32_16x16x32_bf16 v[100:103], v[140:143], v[44:47], v[100:103]\n\t" \
  "buffer_load_dwordx4 v[60:63], v165, %[ro], %[b1] offen offset:1024 sc1\n\t" \
  "ds_read_b64_tr_b16 v[122:123], v160 offset:12800\n\t" \
  "v_mfma_f32_16x16x32_bf16 v[84:87], v[144:147], v[40:43], v[84:87]\n\t" \
  "ds_read_b64_tr_b16 v[124:125], v161 offset:12288\n\t" \
  "v_mfma_f32_16x16x32_bf16 v[104:107], v[144:147], v[44:47], v[104:107]\n\t" \
  "ds_read_b64_tr_b16 v[126:127], v161 offset:12800\n\t" \
  "v_mfma_f32_16x16x32_bf16 v[88:91], v[148:151], v[40:43], v[88:91]\n\t" \
  "ds_read_b64_tr_b16 v[128:129], v162 offset:12288\n\t" \
  "v_mfma_f32_16x16x32_bf16 v[108:111], v[148:151], v[44:47], v[108:111]\n\t" \
  "ds_read_b64_tr_b16 v[130:131], v162 offset:12800\n\t" \
  "s_waitcnt vmcnt(2)\n\t" \
  "v_max3_u32 v168, v48, v49, v50\n\t" \
  "v_mfma_f32_16x16x32_bf16 v[92:95], v[152:155], v[40:43], v[92:95]\n\t" \
  "ds_read_b64_tr_b16 v[132:133], v163 offset:12288\n\t" \
  "v_max3_u32 v169, v51, v52, v53\n\t" \
  "v_mfma_f32_16x16x32_bf16 v[112:115], v[152:155], v[44:47], v[112:115]\n\t" \
  "ds_read_b64_tr_b16 v[134:135], v163 offset:12800\n\t" \
  "v_max3_u32 v168, v168, v54, v55\n\t" \
  "v_mfma_f32_16x16x32_bf16 v[96:99], v[156:159], v[40:43], v[96:99]\n\t" \
  "ds_read_b64_tr_b16 v[136:137], v164 offset:3072\n\t" \
  "v_max_u32 v168, v168, v169\n\t" \
  "v_mfma_f32_16x16x32_bf16 v[116:119], v[156:159], v[44:47], v[116:119]\n\t" \
  "ds_read_b64_tr_b16 v[138:139], v164 offset:3328\n\t" \
  "v_cmp_eq_u32 vcc, -1, v168\n\t" \
  "s_cbranch_vccnz LS6_%=\n\t" \
  "LK6_%=:\n\t" \
  "s_waitcnt lgkmcnt(0)\n\t" \
  "v_mfma_f32_16x16x32_bf16 v[80:83], v[120:123], v[48:51], v[80:83]\n\t" \
  "buffer_load_dwordx4 v[64:67], v165, %[ro], %[a1] offen offset:3072 sc1\n\t" \
  "ds_read_b64_tr_b16 v[140:141], v160 offset:20480\n\t" \
  "v_mfma_f32_16x16x32_bf16 v[100:103], v[120:123], v[52:55], v[100:103]\n\t" \
  "buffer_load_dwordx4 v[68:71], v165, %[ro], %[b1] offen offset:3072 sc1\n\t" \
  "ds_read_b64_tr_b16 v[142:143], v160 offset:20992\n\t" \
  "v_mfma_f32_16x16x32_bf16 v[84:87], v[124:127], v[48:51], v[84:87]\n\t" \
  "ds_read_b64_tr_b16 v[144:145], v161 offset:20480\n\t" \
  "v_mfma_f32_16x16x32_bf16 v[104:107], v[124:127], v[52:55], v[104:107]\n\t" \
  "ds_read_b64_tr_b16 v[146:147], v161 offset:20992\n\t" \
  "v_mfma_f32_16x16x32_bf16 v[88:91], v[128:131], v[48:51], v[88:91]\n\t" \
  "ds_read_b64_tr_b16 v[148:149], v162 offset:20480\n\t" \
  "v_mfma_f32_16x16x32_bf16 v[108:111], v[128:131], v[52:55], v[108:111]\n\t" \
  "ds_read_b64_tr_b16 v[150:151], v162 offset:20992\n\t" \
  "s_waitcnt vmcnt(2)\n\t" \
  "v_max3_u32 v168, v56, v57, v58\n\t" \
  "v_mfma_f32_16x16x32_bf16 v[92:95], v[132:135], v[48:51], v[92:95]\n\t" \
  "ds_read_b64_tr_b16 v[152:153], v163 offset:20480\n\t" \
  "v_max3_u32 v169, v59, v60, v61\n\t" \
  "v_mfma_f32_16x16x32_bf16 v[112:115], v[132:135], v[52:55], v[112:115]\n\t" \
  "ds_read_b64_tr_b16 v[154:155], v163 offset:20992\n\t" \
  "v_max3_u32 v168, v168, v62, v63\n\t" \
  "v_mfma_f32_16x16x32_bf16 v[96:99], v[136:139], v[48:51], v[96:99]\n\t" \
  "ds_read_b64_tr_b16 v[156:157], v164 offset:5120\n\t" \
  "v_max_u32 v168, v168, v169\n\t" \
  "v_mfma_f32_16x16x32_bf16 v[116:119], v[136:139], v[52:55], v[116:119]\n\t" \
  "ds_read_b64_tr_b16 v[158:159], v164 offset:5376\n\t" \
  "v_cmp_eq_u32 vcc, -1, v168\n\t" \
  "s_cbranch_vccnz LS7_%=\n\t" \
  "LK7_%=:\n\t" \
  "s_waitcnt lgkmcnt(0)\n\t" \
  "v_mfma_f32_16x16x32_bf16 v[80:83], v[140:143], v[56:59], v[80:83]\n\t" \
  "buffer_load_dwordx4 v[72:75], v165, %[ro], %[a2] offen offset:1024 sc1\n\t" \
  "ds_read_b64_tr_b16 v[120:121], v160 offset:28672\n\t" \
  "v_mfma_f32_16x16x32_bf16 v[100:103], v[140:143], v[60:63], v[100:103]\n\t" \
  "buffer_load_dwordx4 v[76:79], v165, %[ro], %[b2] offen offset:1024 sc1\n\t" \
  "ds_read_b64_tr_b16 v[122:123], v160 offset:29184\n\t" \
  "v_mfma_f32_16x16x32_bf16 v[84:87], v[144:147], v[56:59], v[84:87]\n\t" \
  "ds_read_b64_tr_b16 v[124:125], v161 offset:28672\n\t" \
  "v_mfma_f32_16x16x32_bf16 v[104:107], v[144:147], v[60:63], v[104:107]\n\t" \
  "ds_read_b64_tr_b16 v[126:127], v161 offset:29184\n\t" \
  "v_mfma_f32_16x16x32_bf16 v[88:91], v[148:151], v[56:59], v[88:91]\n\t" \
  "ds_read_b64_tr_b16 v[128:129], v162 offset:28672\n\t" \
  "v_mfma_f32_16x16x32_bf16 v[108:111], v[148:151], v[60:63], v[108:111]\n\t" \
  "ds_read_b64_tr_b16 v[130:131], v162 offset:29184\n\t" \
  "s_waitcnt vmcnt(2)\n\t" \
  "v_max3_u32 v168, v64, v65, v66\n\t" \
  "v_mfma_f32_16x16x32_bf16 v[92:95], v[152:155], v[56:59], v[92:95]\n\t" \
  "ds_read_b64_tr_b16 v[132:133], v163 offset:28672\n\t" \
  "v_max3_u32 v169, v67, v68, v69\n\t" \
  "v_mfma_f32_16x16x32_bf16 v[112:115], v[152:155], v[60:63], v[112:115]\n\t" \
  "ds_read_b64_tr_b16 v[134:135], v163 offset:29184\n\t" \
  "v_max3_u32 v168, v168, v70, v71\n\t" \
  "v_mfma_f32_16x16x32_bf16 v[96:99], v[156:159], v[56:59], v[96:99]\n\t" \
  "ds_read_b64_tr_b16 v[136:137], v164 offset:7168\n\t" \
  "v_max_u32 v168, v168, v169\n\t" \
  "v_mfma_f32_16x16x32_bf16 v[116:119], v[156:159], v[60:63], v[116:119]\n\t" \
  "ds_read_b64_tr_b16 v[138:139], v164 offset:7424\n\t" \
  "v_cmp_eq_u32 vcc, -1, v168\n\t" \
  "s_cbranch_vccnz LS8_%=\n\t" \
  "LK8_%=:\n\t" \
  "s_waitcnt lgkmcnt(0)\n\t" \
  "v_mfma_f32_16x16x32_bf16 v[80:83], v[120:123], v[64:67], v[80:83]\n\t" \
  "ds_read_b64_tr_b16 v[140:141], v160 offset:36864\n\t" \
  "v_mfma_f32_16x16x32_bf16 v[100:103], v[120:123], v[68:71], v[100:103]\n\t" \
  "ds_read_b64_tr_b16 v[142:143], v160 offset:37376\n\t" \
  "v_mfma_f32_16x16x32_bf16 v[84:87], v[124:127], v[64:67], v[84:87]\n\t" \
  "ds_read_b64_tr_b16 v[144:145], v161 offset:36864\n\t" \
  "v_mfma_f32_16x16x32_bf16 v[104:107], v[124:127], v[68:71], v[104:107]\n\t" \
  "ds_read_b64_tr_b16 v[146:147], v161 offset:37376\n\t" \
  "v_mfma_f32_16x16x32_bf16 v[88:91], v[128:131], v[64:67], v[88:91]\n\t" \
  "ds_read_b64_tr_b16 v[148:149], v162 offset:36864\n\t" \
  "v_mfma_f32_16x16x32_bf16 v[108:111], v[128:131], v[68:71], v[108:111]\n\t" \
  "ds_read_b64_tr_b16 v[150:151], v162 offset:37376\n\t" \
  "s_waitcnt vmcnt(0)\n\t" \
  "v_max3_u32 v168, v72, v73, v74\n\t" \
  "v_mfma_f32_16x16x32_bf16 v[92:95], v[132:135], v[64:67], v[92:95]\n\t" \
  "ds_read_b64_tr_b16 v[152:153], v163 offset:36864\n\t" \
  "v_max3_u32 v169, v75, v76, v77\n\t" \
  "v_mfma_f32_16x16x32_bf16 v[112:115], v[132:135], v[68:71], v[112:115]\n\t" \
  "ds_read_b64_tr_b16 v[154:155], v163 offset:37376\n\t" \
  "v_max3_u32 v168, v168, v78, v79\n\t" \
  "v_mfma_f32_16x16x32_bf16 v[96:99], v[136:139], v[64:67], v[96:99]\n\t" \
  "ds_read_b64_tr_b16 v[156:157], v164 offset:9216\n\t" \
  "v_max_u32 v168, v168, v169\n\t" \
  "v_mfma_f32_16x16x32_bf16 v[116:119], v[136:139], v[68:71], v[116:119]\n\t" \
  "ds_read_b64_tr_b16 v[158:159], v164 offset:9472\n\t" \
  "v_cmp_eq_u32 vcc, -1, v168\n\t" \
  "s_cbranch_vccnz LS9_%=\n\t" \
  "LK9_%=:\n\t" \
  "s_waitcnt lgkmcnt(0)\n\t" \
  "v_mfma_f32_16x16x32_bf16 v[100:103], v[140:143], v[76:79], v[100:103]\n\t" \
  "v_mfma_f32_16x16x32_bf16 v[104:107], v[144:147], v[76:79], v[104:107]\n\t" \
  "v_mfma_f32_16x16x32_bf16 v[108:111], v[148:151], v[76:79], v[108:111]\n\t" \
  "v_mfma_f32_16x16x32_bf16 v[112:115], v[152:155], v[76:79], v[112:115]\n\t" \
  "v_mfma_f32_16x16x32_bf16 v[116:119], v[156:159], v[76:79], v[116:119]\n\t" \
  "v_mfma_f32_16x16x32_bf16 v[80:83], v[140:143], v[72:75], v[80:83]\n\t" \
  "v_mfma_f32_16x16x32_bf16 v[84:87], v[144:147], v[72:75], v[84:87]\n\t" \
  "v_mfma_f32_16x16x32_bf16 v[88:91], v[148:151], v[72:75], v[88:91]\n\t" \
  "v_mfma_f32_16x16x32_bf16 v[92:95], v[152:155], v[72:75], v[92:95]\n\t" \
  "v_mfma_f32_16x16x32_bf16 v[96:99], v[156:159], v[72:75], v[96:99]\n\t" \
  "ds_write_b128 v170, v[100:103]\n\t" \
  "ds_write_b128 v170, v[104:107] offset:1024\n\t" \
  "ds_write_b128 v170, v[108:111] offset:2048\n\t" \
  "ds_write_b128 v170, v[112:115] offset:3072\n\t" \
  "ds_write_b128 v170, v[116:119] offset:4096\n\t" \
  "s_waitcnt lgkmcnt(0)\n\t" \
  "s_barrier\n\t" \
  "ds_read_b128 v[0:3], v171\n\t" \
  "ds_read_b128 v[4:7], v171 offset:1024\n\t" \
  "ds_read_b128 v[8:11], v171 offset:2048\n\t" \
  "ds_read_b128 v[12:15], v171 offset:3072\n\t" \
  "ds_read_b128 v[16:19], v171 offset:4096\n\t" \
  "s_waitcnt lgkmcnt(4)\n\t" \
  "v_pk_add_f32 v[80:81], v[80:81], v[0:1]\n\t" \
  "v_pk_add_f32 v[82:83], v[82:83], v[2:3]\n\t" \
  "v_cvt_pk_bf16_f32 v20, v80, v81\n\t" \
  "v_cvt_pk_bf16_f32 v21, v82, v83\n\t" \
  "s_waitcnt lgkmcnt(3)\n\t" \
  "v_pk_add_f32 v[84:85], v[84:85], v[4:5]\n\t" \
  "v_pk_add_f32 v[86:87], v[86:87], v[6:7]\n\t" \
  "v_cvt_pk_bf16_f32 v22, v84, v85\n\t" \
  "v_cvt_pk_bf16_f32 v23, v86, v87\n\t" \
  "s_waitcnt lgkmcnt(2)\n\t" \
  "v_pk_add_f32 v[88:89], v[88:89], v[8:9]\n\t" \
  "v_pk_add_f32 v[90:91], v[90:91], v[10:11]\n\t" \
  "v_cvt_pk_bf16_f32 v24, v88, v89\n\t" \
  "v_cvt_pk_bf16_f32 v25, v90, v91\n\t" \
  "v_permlane16_swap_b32 v20, v22\n\t" \
  "v_permlane16_swap_b32 v21, v23\n\t" \
  "buffer_store_dwordx4 v[20:23], v172, %[rout], %[so] offen\n\t" \
  "s_waitcnt lgkmcnt(1)\n\t" \
  "v_pk_add_f32 v[92:93], v[92:93], v[12:13]\n\t" \
  "v_pk_add_f32 v[94:95], v[94:95], v[14:15]\n\t" \
  "v_cvt_pk_bf16_f32 v26, v92, v93\n\t" \
  "v_cvt_pk_bf16_f32 v27, v94, v95\n\t" \
  "s_waitcnt lgkmcnt(0)\n\t" \
  "v_pk_add_f32 v[96:97], v[96:97], v[16:17]\n\t" \
  "v_pk_add_f32 v[98:99], v[98:99], v[18:19]\n\t" \
  "v_cvt_pk_bf16_f32 v28, v96, v97\n\t" \
  "v_cvt_pk_bf16_f32 v29, v98, v99\n\t" \
  "v_permlane16_swap_b32 v24, v26\n\t" \
  "v_permlane16_swap_b32 v25, v27\n\t" \
  "buffer_store_dwordx4 v[24:27], v172, %[rout], %[so] offen offset:64\n\t" \
  "buffer_store_dwordx2 v[28:29], v173, %[rout], %[so] offen offset:128\n\t" \
  "s_branch LX_%=\n\t" \
  "LS0_%=:\n\t" \
  "s_waitcnt vmcnt(0)\n\t" \
  "s_getpc_b64 s[94:95]\n\t" \
  "s_add_u32 s94, s94, 12\n\t" \
  "s_addc_u32 s95, s95, 0\n\t" \
  "s_branch LQ0_%=\n\t" \
  "v_max3_u32 v168, v0, v1, v2\n\t" \
  "v_max3_u32 v169, v3, v4, v5\n\t" \
  "v_max3_u32 v168, v168, v6, v7\n\t" \
  "v_max_u32 v168, v168, v169\n\t" \
  "v_cmp_eq_u32 vcc, -1, v168\n\t" \
  "s_cbranch_vccz LK0_%=\n\t" \
  "s_cmp_eq_u32 %[cnt], 0\n\t" \
  "s_cbranch_scc1 LK0_%=\n\t" \
  "s_sub_u32 %[cnt], %[cnt], 1\n\t" \
  "s_branch LQ0_%=\n\t" \
  "LS1_%=:\n\t" \
  "s_waitcnt vmcnt(0)\n\t" \
  "s_getpc_b64 s[94:95]\n\t" \
  "s_add_u32 s94, s94, 12\n\t" \
  "s_addc_u32 s95, s95, 0\n\t" \
  "s_branch LQ1_%=\n\t" \
  "v_max3_u32 v168, v8, v9, v10\n\t" \
  "v_max3_u32 v169, v11, v12, v13\n\t" \
  "v_max3_u32 v168, v168, v14, v15\n\t" \
  "v_max_u32 v168, v168, v169\n\t" \
  "v_cmp_eq_u32 vcc, -1, v168\n\t" \
  "s_cbranch_vccz LK1_%=\n\t" \
  "s_cmp_eq_u32 %[cnt], 0\n\t" \
  "s_cbranch_scc1 LK1_%=\n\t" \
  "s_sub_u32 %[cnt], %[cnt], 1\n\t" \
  "s_branch LQ1_%=\n\t" \
  "LS2_%=:\n\t" \
  "s_waitcnt vmcnt(0)\n\t" \
  "s_getpc_b64 s[94:95]\n\t" \
  "s_add_u32 s94, s94, 12\n\t" \
  "s_addc_u32 s95, s95, 0\n\t" \
  "s_branch LQ2_%=\n\t" \
  "v_max3_u32 v168, v16, v17, v18\n\t" \
  "v_max3_u32 v169, v19, v20, v21\n\t" \
  "v_max3_u32 v168, v168, v22, v23\n\t" \
  "v_max_u32 v168, v168, v169\n\t" \
  "v_cmp_eq_u32 vcc, -1, v168\n\t" \
  "s_cbranch_vccz LK2_%=\n\t" \
  "s_cmp_eq_u32 %[cnt], 0\n\t" \
  "s_cbranch_scc1 LK2_%=\n\t" \
  "s_sub_u32 %[cnt], %[cnt], 1\n\t" \
  "s_branch LQ2_%=\n\t" \
  "LS3_%=:\n\t" \
  "s_waitcnt vmcnt(0)\n\t" \
  "s_getpc_b64 s[94:95]\n\t" \
  "s_add_u32 s94, s94, 12\n\t" \
  "s_addc_u32 s95, s95, 0\n\t" \
  "s_branch LQ3_%=\n\t" \
  "v_max3_u32 v168, v24, v25, v26\n\t" \
  "v_max3_u32 v169, v27, v28, v29\n\t" \
  "v_max3_u32 v168, v168, v30, v31\n\t" \
  "v_max_u32 v168, v168, v169\n\t" \
  "v_cmp_eq_u32 vcc, -1, v168\n\t" \
  "s_cbranch_vccz LK3_%=\n\t" \
  "s_cmp_eq_u32 %[cnt], 0\n\t" \
  "s_cbranch_scc1 LK3_%=\n\t" \
  "s_sub_u32 %[cnt], %[cnt], 1\n\t" \
  "s_branch LQ3_%=\n\t" \
  "LS4_%=:\n\t" \
  "s_waitcnt vmcnt(0)\n\t" \
  "s_getpc_b64 s[94:95]\n\t" \
  "s_add_u32 s94, s94, 12\n\t" \
  "s_addc_u32 s95, s95, 0\n\t" \
  "s_branch LQ4_%=\n\t" \
  "v_max3_u32 v168, v32, v33, v34\n\t" \
  "v_max3_u32 v169, v35, v36, v37\n\t" \
  "v_max3_u32 v168, v168, v38, v39\n\t" \
  "v_max_u32 v168, v168, v169\n\t" \
  "v_cmp_eq_u32 vcc, -1, v168\n\t" \
  "s_cbranch_vccz LK4_%=\n\t" \
  "s_cmp_eq_u32 %[cnt], 0\n\t" \
  "s_cbranch_scc1 LK4_%=\n\t" \
  "s_sub_u32 %[cnt], %[cnt], 1\n\t" \
  "s_branch LQ4_%=\n\t" \
  "LS5_%=:\n\t" \
  "s_waitcnt vmcnt(0)\n\t" \
  "s_getpc_b64 s[94:95]\n\t" \
  "s_add_u32 s94, s94, 12\n\t" \
  "s_addc_u32 s95, s95, 0\n\t" \
  "s_branch LQ5_%=\n\t" \
  "v_max3_u32 v168, v40, v41, v42\n\t" \
  "v_max3_u32 v169, v43, v44, v45\n\t" \
  "v_max3_u32 v168, v168, v46, v47\n\t" \
  "v_max_u32 v168, v168, v169\n\t" \
  "v_cmp_eq_u32 vcc, -1, v168\n\t" \
  "s_cbranch_vccz LK5_%=\n\t" \
  "s_cmp_eq_u32 %[cnt], 0\n\t" \
  "s_cbranch_scc1 LK5_%=\n\t" \
  "s_sub_u32 %[cnt], %[cnt], 1\n\t" \
  "s_branch LQ5_%=\n\t" \
  "LS6_%=:\n\t" \
  "s_waitcnt vmcnt(0)\n\t" \
  "s_getpc_b64 s[94:95]\n\t" \
  "s_add_u32 s94, s94, 12\n\t" \
  "s_addc_u32 s95, s95, 0\n\t" \
  "s_branch LQ6_%=\n\t" \
  "v_max3_u32 v168, v48, v49, v50\n\t" \
  "v_max3_u32 v169, v51, v52, v53\n\t" \
  "v_max3_u32 v168, v168, v54, v55\n\t" \
  "v_max_u32 v168, v168, v169\n\t" \
  "v_cmp_eq_u32 vcc, -1, v168\n\t" \
  "s_cbranch_vccz LK6_%=\n\t" \
  "s_cmp_eq_u32 %[cnt], 0\n\t" \
  "s_cbranch_scc1 LK6_%=\n\t" \
  "s_sub_u32 %[cnt], %[cnt], 1\n\t" \
  "s_branch LQ6_%=\n\t" \
  "LS7_%=:\n\t" \
  "s_waitcnt vmcnt(0)\n\t" \
  "s_getpc_b64 s[94:95]\n\t" \
  "s_add_u32 s94, s94, 12\n\t" \
  "s_addc_u32 s95, s95, 0\n\t" \
  "s_branch LQ7_%=\n\t" \
  "v_max3_u32 v168, v56, v57, v58\n\t" \
  "v_max3_u32 v169, v59, v60, v61\n\t" \
  "v_max3_u32 v168, v168, v62, v63\n\t" \
  "v_max_u32 v168, v168, v169\n\t" \
  "v_cmp_eq_u32 vcc, -1, v168\n\t" \
  "s_cbranch_vccz LK7_%=\n\t" \
  "s_cmp_eq_u32 %[cnt], 0\n\t" \
  "s_cbranch_scc1 LK7_%=\n\t" \
  "s_sub_u32 %[cnt], %[cnt], 1\n\t" \
  "s_branch LQ7_%=\n\t" \
  "LS8_%=:\n\t" \
  "s_waitcnt vmcnt(0)\n\t" \
  "s_getpc_b64 s[94:95]\n\t" \
  "s_add_u32 s94, s94, 12\n\t" \
  "s_addc_u32 s95, s95, 0\n\t" \
  "s_branch LQ8_%=\n\t" \
  "v_max3_u32 v168, v64, v65, v66\n\t" \
  "v_max3_u32 v169, v67, v68, v69\n\t" \
  "v_max3_u32 v168, v168, v70, v71\n\t" \
  "v_max_u32 v168, v168, v169\n\t" \
  "v_cmp_eq_u32 vcc, -1, v168\n\t" \
  "s_cbranch_vccz LK8_%=\n\t" \
  "s_cmp_eq_u32 %[cnt], 0\n\t" \
  "s_cbranch_scc1 LK8_%=\n\t" \
  "s_sub_u32 %[cnt], %[cnt], 1\n\t" \
  "s_branch LQ8_%=\n\t" \
  "LS9_%=:\n\t" \
  "s_waitcnt vmcnt(0)\n\t" \
  "s_getpc_b64 s[94:95]\n\t" \
  "s_add_u32 s94, s94, 12\n\t" \
  "s_addc_u32 s95, s95, 0\n\t" \
  "s_branch LQ9_%=\n\t" \
  "v_max3_u32 v168, v72, v73, v74\n\t" \
  "v_max3_u32 v169, v75, v76, v77\n\t" \
  "v_max3_u32 v168, v168, v78, v79\n\t" \
  "v_max_u32 v168, v168, v169\n\t" \
  "v_cmp_eq_u32 vcc, -1, v168\n\t" \
  "s_cbranch_vccz LK9_%=\n\t" \
  "s_cmp_eq_u32 %[cnt], 0\n\t" \
  "s_cbranch_scc1 LK9_%=\n\t" \
  "s_sub_u32 %[cnt], %[cnt], 1\n\t" \
  "s_branch LQ9_%=\n\t" \
  "LQ0_%=:\n\t" \
  "v_max3_u32 v168, v0, v1, v2\n\t" \
  "v_max3_u32 v169, v3, v4, v5\n\t" \
  "v_max3_u32 v168, v168, v6, v7\n\t" \
  "v_max_u32 v168, v168, v169\n\t" \
  "v_cmp_eq_u32 vcc, -1, v168\n\t" \
  "s_cbranch_vccz LW0_%=\n\t" \
  "buffer_load_dwordx4 v[0:3], v165, %[ro], %[a0] offen sc1\n\t" \
  "buffer_load_dwordx4 v[4:7], v165, %[ro], %[b0] offen sc1\n\t" \
  "LW0_%=:\n\t" \
  "LQ1_%=:\n\t" \
  "v_max3_u32 v168, v8, v9, v10\n\t" \
  "v_max3_u32 v169, v11, v12, v13\n\t" \
  "v_max3_u32 v168, v168, v14, v15\n\t" \
  "v_max_u32 v168, v168, v169\n\t" \
  "v_cmp_eq_u32 vcc, -1, v168\n\t" \
  "s_cbranch_vccz LW1_%=\n\t" \
  "buffer_load_dwordx4 v[8:11], v165, %[ro], %[a0] offen offset:2048 sc1\n\t" \
  "buffer_load_dwordx4 v[12:15], v165, %[ro], %[b0] offen offset:2048 sc1\n\t" \
  "LW1_%=:\n\t" \
  "LQ2_%=:\n\t" \
  "v_max3_u32 v168, v16, v17, v18\n\t" \
  "v_max3_u32 v169, v19, v20, v21\n\t" \
  "v_max3_u32 v168, v168, v22, v23\n\t" \
  "v_max_u32 v168, v168, v169\n\t" \
  "v_cmp_eq_u32 vcc, -1, v168\n\t" \
  "s_cbranch_vccz LW2_%=\n\t" \
  "buffer_load_dwordx4 v[16:19], v165, %[ro], %[a1] offen sc1\n\t" \
  "buffer_load_dwordx4 v[20:23], v165, %[ro], %[b1] offen sc1\n\t" \
  "LW2_%=:\n\t" \
  "LQ3_%=:\n\t" \
  "v_max3_u32 v168, v24, v25, v26\n\t" \
  "v_max3_u32 v169, v27, v28, v29\n\t" \
  "v_max3_u32 v168, v168, v30, v31\n\t" \
  "v_max_u32 v168, v168, v169\n\t" \
  "v_cmp_eq_u32 vcc, -1, v168\n\t" \
  "s_cbranch_vccz LW3_%=\n\t" \
  "buffer_load_dwordx4 v[24:27], v165, %[ro], %[a1] offen offset:2048 sc1\n\t" \
  "buffer_load_dwordx4 v[28:31], v165, %[ro], %[b1] offen offset:2048 sc1\n\t" \
  "LW3_%=:\n\t" \
  "LQ4_%=:\n\t" \
  "v_max3_u32 v168, v32, v33, v34\n\t" \
  "v_max3_u32 v169, v35, v36, v37\n\t" \
  "v_max3_u32 v168, v168, v38, v39\n\t" \
  "v_max_u32 v168, v168, v169\n\t" \
  "v_cmp_eq_u32 vcc, -1, v168\n\t" \
  "s_cbranch_vccz LW4_%=\n\t" \
  "buffer_load_dwordx4 v[32:35], v165, %[ro], %[a2] offen sc1\n\t" \
  "buffer_load_dwordx4 v[36:39], v165, %[ro], %[b2] offen sc1\n\t" \
  "LW4_%=:\n\t" \
  "LQ5_%=:\n\t" \
  "v_max3_u32 v168, v40, v41, v42\n\t" \
  "v_max3_u32 v169, v43, v44, v45\n\t" \
  "v_max3_u32 v168, v168, v46, v47\n\t" \
  "v_max_u32 v168, v168, v169\n\t" \
  "v_cmp_eq_u32 vcc, -1, v168\n\t" \
  "s_cbranch_vccz LW5_%=\n\t" \
  "buffer_load_dwordx4 v[40:43], v165, %[ro], %[a0] offen offset:1024 sc1\n\t" \
  "buffer_load_dwordx4 v[44:47], v165, %[ro], %[b0] offen offset:1024 sc1\n\t" \
  "LW5_%=:\n\t" \
  "LQ6_%=:\n\t" \
  "v_max3_u32 v168, v48, v49, v50\n\t" \
  "v_max3_u32 v169, v51, v52, v53\n\t" \
  "v_max3_u32 v168, v168, v54, v55\n\t" \
  "v_max_u32 v168, v168, v169\n\t" \
  "v_cmp_eq_u32 vcc, -1, v168\n\t" \
  "s_cbranch_vccz LW6_%=\n\t" \
  "buffer_load_dwordx4 v[48:51], v165, %[ro], %[a0] offen offset:3072 sc1\n\t" \
  "buffer_load_dwordx4 v[52:55], v165, %[ro], %[b0] offen offset:3072 sc1\n\t" \
  "LW6_%=:\n\t" \
  "LQ7_%=:\n\t" \
  "v_max3_u32 v168, v56, v57, v58\n\t" \
  "v_max3_u32 v169, v59, v60, v61\n\t" \
  "v_max3_u32 v168, v168, v62, v63\n\t" \
  "v_max_u32 v168, v168, v169\n\t" \
  "v_cmp_eq_u32 vcc, -1, v168\n\t" \
  "s_cbranch_vccz LW7_%=\n\t" \
  "buffer_load_dwordx4 v[56:59], v165, %[ro], %[a1] offen offset:1024 sc1\n\t" \
  "buffer_load_dwordx4 v[60:63], v165, %[ro], %[b1] offen offset:1024 sc1\n\t" \
  "LW7_%=:\n\t" \
  "LQ8_%=:\n\t" \
  "v_max3_u32 v168, v64, v65, v66\n\t" \
  "v_max3_u32 v169, v67, v68, v69\n\t" \
  "v_max3_u32 v168, v168, v70, v71\n\t" \
  "v_max_u32 v168, v168, v169\n\t" \
  "v_cmp_eq_u32 vcc, -1, v168\n\t" \
  "s_cbranch_vccz LW8_%=\n\t" \
  "buffer_load_dwordx4 v[64:67], v165, %[ro], %[a1] offen offset:3072 sc1\n\t" \
  "buffer_load_dwordx4 v[68:71], v165, %[ro], %[b1] offen offset:3072 sc1\n\t" \
  "LW8_%=:\n\t" \
  "LQ9_%=:\n\t" \
  "v_max3_u32 v168, v72, v73, v74\n\t" \
  "v_max3_u32 v169, v75, v76, v77\n\t" \
  "v_max3_u32 v168, v168, v78, v79\n\t" \
  "v_max_u32 v168, v168, v169\n\t" \
  "v_cmp_eq_u32 vcc, -1, v168\n\t" \
  "s_cbranch_vccz LW9_%=\n\t" \
  "buffer_load_dwordx4 v[72:75], v165, %[ro], %[a2] offen offset:1024 sc1\n\t" \
  "buffer_load_dwordx4 v[76:79], v165, %[ro], %[b2] offen offset:1024 sc1\n\t" \
  "LW9_%=:\n\t" \
  "s_waitcnt vmcnt(0)\n\t" \
  "s_setpc_b64 s[94:95]\n\t" \
  "LX_%=:\n\t"

DI float rowsum_of(const f32x16& o) {
  auto pr = __builtin_amdgcn_permlane32_swap(__float_as_uint(o[8]), __float_as_uint(o[8]), false, false);
  return __uint_as_float(pr[1]);
}
DI float sum12(const f32x16& o) {
  const f32x2 a = f32x2{o[0], o[1]} + f32x2{o[2], o[3]}, b = f32x2{o[4], o[5]} + f32x2{o[6], o[7]}, c = f32x2{o[8], o[9]} + f32x2{o[10], o[11]};
  const f32x2 d = (a + b) + c;
  return d[0] + d[1];
}
DI bool bad_sum(float lsum) { return !(lsum >= 8.0e-25f && lsum <= 1.0e30f); }

DI void attn_safe(__amdgpu_buffer_rsrc_t rkv, unsigned o1, unsigned o2, unsigned ov, u32x4 qa1, u32x2 qa2, u32x4 qb1, u32x2 qb2,
                  unsigned h, f32x16& oa, f32x16& ob) {
  const f32x16 z = {};
  if (h) { qa2[0] = 0; qb2[0] = 0; }
  oa = z; ob = z;
  float ma = -3.0e38f, mb = -3.0e38f;
#pragma unroll 1
  for (unsigned t = 0; t < 64; ++t) {
    const unsigned odd = t & 1u;
    const unsigned so = (t >> 3) * CH + ((t >> 1) & 3u) * PAIR_B;
    const u32x4 k1 = gld4(rkv, o1, so + odd * 1024u), k2p = gld4(rkv, o2, so);
    const u32x4 v0 = gld4(rkv, ov, so + odd * 1408u), v1 = gld4(rkv, ov, so + odd * 1408u + 704u);
    const u32x2 k2 = odd ? u32x2{k2p[2], k2p[3]} : u32x2{k2p[0], k2p[1]};
    f32x16 sa = mfma8(as4(k2), as4(qa2), mfma16(as8(k1), as8(qa1), z)), sb = mfma8(as4(k2), as4(qb2), mfma16(as8(k1), as8(qb1), z));
    float xa = ma, xb = mb;
#pragma unroll
    for (int i = 0; i < 16; ++i) { xa = fmaxf(xa, sa[i]); xb = fmaxf(xb, sb[i]); }
    xa = halfmax(xa); xb = halfmax(xb);
    const float fa = ex2(ma - xa), fb = ex2(mb - xb);
    ma = xa; mb = xb;
#pragma unroll
    for (int i = 0; i < 16; ++i) { sa[i] = ex2(sa[i] - xa); sb[i] = ex2(sb[i] - xb); oa[i] *= fa; ob[i] *= fb; }
    oa = spv(v0, sa, 0, oa); oa = spv(v1, sa, 8, oa);
    ob = spv(v0, sb, 0, ob); ob = spv(v1, sb, 8, ob);
  }
}

__global__ __launch_bounds__(512, 2) void k_mha2(const char* __restrict__ xA, const char* __restrict__ wA, const char* __restrict__ woA,
                                                 const char* __restrict__ xB, const char* __restrict__ wB, const char* __restrict__ woB,
                                                 char* __restrict__ out, char* __restrict__ ws) {
  extern __shared__ __attribute__((aligned(128))) char lds[];
  const unsigned lb = (unsigned)(size_t)(LDSP(char))lds;
  const unsigned l = threadIdx.x & 63u, w = rfl(threadIdx.x >> 6), h = l >> 5, g = l >> 4, i16 = l & 15u;
  unsigned p, hp, e, tile3, col3; decode(blockIdx.x, p, hp, e, tile3, col3);
  const unsigned tq = w & 3u;
  const __amdgpu_buffer_rsrc_t rx = mkrsrc(p ? xB : xA, SEQ * 1280), rw = mkrsrc(p ? wB : wA, 640 * 3840);
  const __amdgpu_buffer_rsrc_t rfl_ = mkrsrc(ws + WS_O, OF_BYTES);
  const unsigned rot = rfl(((e & 1u) * 5u + (hp & 1u) * 2u));
  const unsigned vx = (8 * w + (l >> 3)) * 1280u + (((l & 7u) ^ ((4u * (w & 1u) + (l >> 4)) & 7u)) * 16u);
  unsigned vw;
  { const unsigned P = i16 ^ (((((w >> 1) & 1u) << 2) | g) << 1);
    const unsigned co = 16u * P + 1192u * (((P + 11u) >> 4) + ((P + 5u) >> 4));
    vw = (4 * w + g) * 3840u + hp * 80u + co; }
  const unsigned lw = lb + w * 1024u, sx0 = e * 256u * 1280u;
  { const unsigned kq = rot + 0 >= 10 ? rot + 0 - 10 : rot + 0;
    dma16(lw + 0 * P1_STAGE + 0, vx, rx, sx0 + 0 + kq * 128u);
    dma16(lw + 0 * P1_STAGE + 8192, vx, rx, sx0 + 81920 + kq * 128u);
    dma16(lw + 0 * P1_STAGE + 16384, vx, rx, sx0 + 163840 + kq * 128u);
    dma16(lw + 0 * P1_STAGE + 24576, vx, rx, sx0 + 245760 + kq * 128u);
    dma16(lw + 0 * P1_STAGE + 32768, vw, rw, kq * 245760u + 0);
    dma16(lw + 0 * P1_STAGE + 40960, vw, rw, kq * 245760u + 122880);
  }

  unsigned ll = l;
  asm volatile("" : "+v"(ll));
  const unsigned hd = w >> 2;
  const unsigned img = hd * CH_HEAD + tq * PAIR_B;
  const unsigned o1 = hd * CH_HEAD + ll * 16u;
  const unsigned o2 = hd * CH_HEAD + (ll >= 32u ? 2560u + 320u : 2048u + ll * 16u);
  const unsigned ov = hd * CH_HEAD + 2560u + (ll >> 5) * 352u + ((ll & 31u) > 21u ? 21u : (ll & 31u)) * 16u;
  u32x8 g1, g4; u32x16 g2, g3;
  {
    const unsigned h = ll >> 5, g = ll >> 4, i16 = ll & 15u, l = ll;
    const unsigned q = (l >> 2) & 3u, c = l & 3u, idx = ((g & 1u) << 2) | q;
    const unsigned ax0 = lb + (64 * tq + i16) * 128u + ((g ^ ((i16 >> 1) & 7u)) * 16u);
    const unsigned s0 = c + 5 * hd, s1 = 11 + c + 5 * hd;
    const unsigned s2 = ((0x19160F04u >> (8 * c)) & 255u) + 5 * hd, s3 = ((0x191A1817u >> (8 * c)) & 255u) + 5 * hd;
    const unsigned wb = lb + 32768 + (8 * g + q) * 256u;
    g1 = u32x8{ax0, ax0 ^ 64u, wb + (((s0 >> 1) ^ (idx << 1)) * 16u) + (s0 & 1u) * 8u, wb + (((s1 >> 1) ^ (idx << 1)) * 16u) + (s1 & 1u) * 8u,
               wb + (((s2 >> 1) ^ (idx << 1)) * 16u) + (s2 & 1u) * 8u, wb + (((s3 >> 1) ^ (idx << 1)) * 16u) + (s3 & 1u) * 8u, vx, vw};
    { const unsigned j = l >> 3, sw = 2u * ((j >> 1) & 1u) + 4u * (w & 1u), ca = col3 * 160u + ((l & 7u) ^ sw) * 16u;
      g2[0] = j * 1280u + ca;
      g2[1] = (j + 16u * (j >> 2)) * 1280u + ca;
      const unsigned rr = l >> 1, k = (rr & ~12u) | ((rr & 4u) << 1) | ((rr & 8u) >> 1), m = k >> 3, jb = k & 7u, cb = col3 * 160u + 128u + (l & 1u) * 16u;
      g2[2] = (20u * (m >> 1) + 8u * (m & 1u) + jb) * 1280u + cb;
      g2[3] = (80u * m + 16u + jb + 16u * (jb >> 2)) * 1280u + cb; }
    const unsigned row = 16u * ((l >> 3) & 1u) + 8u * ((l >> 2) & 1u) + 4u * ((l >> 4) & 1u) + (l & 3u);
    const unsigned ok1 = h * 512u + row * 16u, ov1 = 2560u + (g >> 1) * 704u + (g & 1u) * 352u + i16 * 16u;
    const unsigned lim = lb + 2 * CH + img;
    g2[4] = (l & 7u) * 8u; g2[5] = l * 16u; g2[6] = 0; g2[7] = 0; g2[8] = 0; g2[9] = 0;
    g2[10] = lb + 2 * CH + o1; g2[11] = lb + 2 * CH + o2; g2[12] = lb + 2 * CH + ov;
    g2[13] = ok1; g2[14] = ov1; g2[15] = lim + ok1;
    const unsigned k1 = ((0x180C0410u >> (8 * (i16 >> 2))) & 255u) + (i16 & 3u);
    const unsigned k2 = i16 < 4u ? 20u + i16 : 255u;
#pragma unroll
    for (unsigned r = 0; r < 4; ++r) {
      const unsigned k = 8 * g + 2 * r;
      g3[r] = (k1 == k ? 0x3F80u : 0u) | (k1 == k + 1 ? 0x3F800000u : 0u);
      g3[4 + r] = (k2 == k ? 0x3F80u : 0u) | (k2 == k + 1 ? 0x3F800000u : 0u);
      g3[8 + r] = i16 == 4u ? 0x3F800000u : 0u;
    }
    g3[12] = __builtin_bit_cast(unsigned, g == 0 ? QSCALE : 1.0f); g3[13] = lim + ov1; g3[14] = 0; g3[15] = 0;
    const unsigned tok = e * 256 + tq * 64 + (l & 31u), ob_ = (tok >> 4) * 20480u + (tok & 15u) * 16u;
    g4 = u32x8{ob_ + h * 256u, ob_ + h * 2 * 20480u, i16 * 8u, 0, EMPTY, EMPTY, EMPTY, EMPTY};
  }
  const __amdgpu_buffer_rsrc_t rkv = mkrsrc(ws + WS_KV + (size_t)(p * 16 + hp) * 8 * CH, 8 * CH);
  const __amdgpu_buffer_rsrc_t rwo = mkrsrc(p ? woB : woA, 640 * 1280);
  const unsigned so1 = p * O_PROB + (4 * hp + 2 * hd) * 256u, so2 = p * O_PROB + (64u + (hp & 8u) + ((hp & 1u) << 2) + ((hp & 7u) >> 1)) * 256u + hd * 8u;
  f32x16 oa, ob;
  u32x16 qq;
  {
    unsigned kq = rfl(rot + 1 >= 10 ? rot + 1 - 10 : rot + 1);
    unsigned st, cur, nxt, dl, tmp, wsh, lsh;
    unsigned mb, sx, sw;
    const unsigned long long qs = __builtin_bit_cast(unsigned, QSCALE) * 0x100000001ull, m6 = 0x003F003F003F003Full;
    asm volatile(MAIN_ASM
      : "=&{v[136:151]}"(oa), "=&{v[152:167]}"(ob), "=&{v[216:231]}"(qq), "+{v[128:135]}"(g1), "+{v[184:199]}"(g2), "+{v[232:239]}"(g4),
        [kq] "+s"(kq), [st] "=&s"(st), [cur] "=&s"(cur), [nxt] "=&s"(nxt), [dl] "=&s"(dl), [mb] "=&s"(mb), [sx] "=&s"(sx), [sw] "=&s"(sw), [tmp] "=&s"(tmp), [wsh] "=&s"(wsh), [lsh] "=&s"(lsh)
      : "{v[200:215]}"(g3), [rx] "s"(rx), [rw] "s"(rw), [lw] "s"(lw), [sx0] "s"(sx0),
        [gim] "s"(e * CH + img), [qs] "s"(qs), [m6] "s"(m6), [fe] "s"(FL_E + p * 128u), [so1] "s"(so1), [so2] "s"(so2),
        [fo2] "s"(FL_O + (p * 8 + e) * 128u + hp * 8u), [fg2] "s"(FL_O + (p * 8 + (tile3 >> 1)) * 128u),
        [sa1] "s"((40u * (w >> 2) + 20u * ((w >> 1) & 1u) + 8u * (w & 1u)) * 1280u), [sa2] "s"((40u * (2u * (w & 3u) + (w >> 2)) + 16u) * 1280u),
        [sb1] "s"(w * 51200u), [sb2] "s"((((w & 3u) >> 1) * 320u + (w & 1u) * 40u) * 1280u), [rkv] "s"(rkv), [rwo] "s"(rwo), [rfl] "s"(rfl_), [w1k] "s"(w * 1024u), [e1] "s"(e + 1),
        [fo] "s"(FL_K + (p * 16 + hp) * 128u + e * 8u), [fg] "s"(FL_K + (p * 16 + hp) * 128u), [lim] "s"(SPIN_LIMIT)
      : "memory", "scc", "vcc", "v0", "v1", "v2", "v3", "v4", "v5", "v6", "v7", "v8", "v9", "v10", "v11", "v12", "v13", "v14", "v15", "v16", "v17", "v18", "v19", "v20", "v21", "v22", "v23", "v24", "v25", "v26", "v27", "v28", "v29", "v30", "v31", "v32", "v33", "v34", "v35", "v36", "v37", "v38", "v39", "v40", "v41", "v42", "v43", "v44", "v45", "v46", "v47", "v48", "v49", "v50", "v51", "v52", "v53", "v54", "v55", "v56", "v57", "v58", "v59", "v60", "v61", "v62", "v63", "v64", "v65", "v66", "v67", "v68", "v69", "v70", "v71", "v72", "v73", "v74", "v75", "v76", "v77", "v78", "v79", "v80", "v81", "v82", "v83", "v84", "v85", "v86", "v87", "v88", "v89", "v90", "v91", "v92", "v93", "v94", "v95", "v96", "v97", "v98", "v99", "v100", "v101", "v102", "v103", "v104", "v105", "v106", "v107", "v108", "v109", "v110", "v111", "v112", "v113", "v114", "v115", "v116", "v117", "v118", "v119", "v120", "v121", "v122", "v123", "v124", "v125", "v126", "v127", "v168", "v169", "v170", "v171", "v172", "v173", "v174", "v175", "v176", "v177", "v178", "v179", "v180", "v181", "v182", "v183");
  }
  const u32x2 tag = {g2[8], g2[9]};
  float la = rowsum_of(oa), lbs = rowsum_of(ob);
  const bool redo = __any((int)(bad_sum(la) || bad_sum(lbs) || !(fabsf(sum12(oa) + sum12(ob)) <= 3.0e38f)));
  if (redo) {
    attn_safe(rkv, o1, o2, ov, u32x4{qq[0], qq[1], qq[2], qq[3]}, u32x2{qq[4], qq[5]}, u32x4{qq[6], qq[7], qq[8], qq[9]}, u32x2{qq[10], qq[11]}, h, oa, ob);
    la = rowsum_of(oa); lbs = rowsum_of(ob);
  }
  {
    const float ra = __builtin_amdgcn_rcpf(la), rb = __builtin_amdgcn_rcpf(lbs);
    u32x2 xa = {cvtpk(oa[0] * ra, oa[1] * ra), cvtpk(oa[2] * ra, oa[3] * ra)}, ya = {cvtpk(oa[4] * ra, oa[5] * ra), cvtpk(oa[6] * ra, oa[7] * ra)};
    u32x2 xb = {cvtpk(ob[0] * rb, ob[1] * rb), cvtpk(ob[2] * rb, ob[3] * rb)}, yb = {cvtpk(ob[4] * rb, ob[5] * rb), cvtpk(ob[6] * rb, ob[7] * rb)};
    u32x2 za = {cvtpk(oa[8] * ra, oa[9] * ra), cvtpk(oa[10] * ra, oa[11] * ra)}, zb = {cvtpk(ob[8] * rb, ob[9] * rb), cvtpk(ob[10] * rb, ob[11] * rb)};
    if (redo) {
      auto fix = [](u32x2& v) { v[0] = min(v[0], EMPTY - 1u); v[1] = min(v[1], EMPTY - 1u); };
      fix(xa); fix(ya); fix(xb); fix(yb); fix(za); fix(zb);
    }
    swap32(xa, ya); swap32(xb, yb); swap32(za, zb);
    gst4(cat(xa, ya), rfl_, g4[0], so1);
    gst4(cat(xb, yb), rfl_, g4[0], so1 + 2 * 20480u);
    gst2(za, rfl_, g4[1], so2);
  }
  if (__any((int)(g2[6] != tag[0] || g2[7] != tag[1]))) {
    unsigned cnt = SPIN_LIMIT;
#pragma unroll 1
    while (true) {
      const u32x2 f = gld2(rfl_, i16 * 8u, FL_O + (p * 8 + (tile3 >> 1)) * 128u);
      if (!__any((int)(f[0] != tag[0] || f[1] != tag[1])) || --cnt == 0) break;
      __builtin_amdgcn_s_sleep(1);
    }
    asm volatile("" ::: "memory");
  }
  {
    const unsigned tp = w & 3u, kh = w >> 2;
    const unsigned ob0 = p * O_PROB + (tile3 * 8 + tp * 2) * 20480u + kh * 10240u;
    const __amdgpu_buffer_rsrc_t rout = mkrsrc(out, SEQ * 2560);
    unsigned t0_, t1_, t2_, t3_, t4_;
    asm volatile(P3_ASM
      : [a1] "=&s"(t0_), [a2] "=&s"(t1_), [b1] "=&s"(t2_), [b2] "=&s"(t3_), [cnt] "=&s"(t4_)
      : "{v165}"(g2[5]), [ro] "s"(rfl_), [a0] "s"(ob0 + kh * 20480u), [b0] "s"(ob0 + (kh ^ 1u) * 20480u), [lim] "s"(SPIN_LIMIT),
        [la] "s"(lb + CH + kh * 40960u), [lb4] "s"(lb + CH + 81920u + kh * 10240u), [lm] "s"(lb + w * 5120u), [lo] "s"(lb + (w ^ 4u) * 5120u),
        [rout] "s"(rout), [so] "s"((tile3 * 128 + tp * 32 + kh * 16) * 2560u + p * 1280u + col3 * 160u)
      : "memory", "scc", "vcc", "s94", "s95", "v0", "v1", "v2", "v3", "v4", "v5", "v6", "v7", "v8", "v9", "v10", "v11", "v12", "v13", "v14", "v15", "v16", "v17", "v18", "v19", "v20", "v21", "v22", "v23", "v24", "v25", "v26", "v27", "v28", "v29", "v30", "v31", "v32", "v33", "v34", "v35", "v36", "v37", "v38", "v39", "v40", "v41", "v42", "v43", "v44", "v45", "v46", "v47", "v48", "v49", "v50", "v51", "v52", "v53", "v54", "v55", "v56", "v57", "v58", "v59", "v60", "v61", "v62", "v63", "v64", "v65", "v66", "v67", "v68", "v69", "v70", "v71", "v72", "v73", "v74", "v75", "v76", "v77", "v78", "v79", "v80", "v81", "v82", "v83", "v84", "v85", "v86", "v87", "v88", "v89", "v90", "v91", "v92", "v93", "v94", "v95", "v96", "v97", "v98", "v99", "v100", "v101", "v102", "v103", "v104", "v105", "v106", "v107", "v108", "v109", "v110", "v111", "v112", "v113", "v114", "v115", "v116", "v117", "v118", "v119", "v120", "v121", "v122", "v123", "v124", "v125", "v126", "v127", "v128", "v129", "v130", "v131", "v132", "v133", "v134", "v135", "v136", "v137", "v138", "v139", "v140", "v141", "v142", "v143", "v144", "v145", "v146", "v147", "v148", "v149", "v150", "v151", "v152", "v153", "v154", "v155", "v156", "v157", "v158", "v159", "v160", "v161", "v162", "v163", "v164", "v168", "v169", "v170", "v171", "v172", "v173", "v174", "v175");

    if (tile3 == 0 && col3 == 0 && threadIdx.x == 0) gst2(tag, rfl_, 0, FL_E + p * 128u);
  }
}

}

extern "C" void kernel_launch(void* const* d_in, const int* in_sizes, int n_in, void* d_out, int out_size, void* d_ws, size_t ws_size,
                              hipStream_t stream) {
  (void)in_sizes; (void)n_in; (void)out_size; (void)ws_size;
  (void)hipFuncSetAttribute((const void*)k_mha2, hipFuncAttributeMaxDynamicSharedMemorySize, LDS_BYTES);
  k_mha2<<<256, 512, LDS_BYTES, stream>>>((const char*)d_in[0], (const char*)d_in[1], (const char*)d_in[2], (const char*)d_in[3],
                                          (const char*)d_in[4], (const char*)d_in[5], (char*)d_out, (char*)d_ws);
}
```
